```python
import torch

def _softmax(x, axis=-1):
    return torch.softmax(x, dim=axis)
def _gelu(x):
    return torch.nn.functional.gelu(x, approximate="tanh")
def _silu(x):
    return torch.nn.functional.silu(x)
def _sigmoid(x):
    return torch.sigmoid(x)
def _layernorm(x, axis=-1, eps=1e-5):
    m = x.mean(dim=axis, keepdim=True)
    v = x.var(dim=axis, keepdim=True, unbiased=False)
    return (x - m) / torch.sqrt(v + eps)
def _scatter_add(tgt, idx, src, axis=0):
    return tgt.clone().index_add_(axis, idx, src)
def _segment_sum(src, seg, n):
    out = torch.zeros((n,) + src.shape[1:], dtype=src.dtype)
    return out.index_add_(0, seg, src)
def _pad(x, pw):
    flat = tuple(v for pair in reversed(pw) for v in pair)
    return torch.nn.functional.pad(x, flat)

kernel_name = 'interleave_layers__linear_attn_block__grouped_kv_mqa__prompt'

def setup_inputs():

    _w = (lambda a: (lambda t: t.to(torch.bfloat16) if t.is_floating_point() else t)(torch.as_tensor(a)))
    def _A_setup():
        B, S, D = 1, 2048, 512
        return {
            "x": _w((lambda *s: torch.randn(*s, dtype=torch.bfloat16) * 0.05)(B, S, D)),
            "wqkv": _w((lambda *s: torch.randn(*s, dtype=torch.bfloat16) * 0.05)(D, 3 * D)),
            "wo": _w((lambda *s: torch.randn(*s, dtype=torch.bfloat16) * 0.05)(D, D)),
        }
    _A_in = _A_setup()

    def _B_setup():
        B, S, D, H = 1, 2048, 512, 32
        Dh = D // H
        return {
            "x": _w((lambda *s: torch.randn(*s, dtype=torch.bfloat16) * 0.05)(B, S, D)),
            "wq": _w((lambda *s: torch.randn(*s, dtype=torch.bfloat16) * 0.1)(D, H * Dh)),
            "wk": _w((lambda *s: torch.randn(*s, dtype=torch.bfloat16) * 0.1)(D, Dh)),
            "wv": _w((lambda *s: torch.randn(*s, dtype=torch.bfloat16) * 0.05)(D, Dh)),
            "wo": _w((lambda *s: torch.randn(*s, dtype=torch.bfloat16) * 0.05)(H * Dh, D)),
        }
    _B_in = _B_setup()

    return {
        "A_x": _A_in["x"],
        "A_wqkv": _A_in["wqkv"],
        "A_wo": _A_in["wo"],
        "B_x": _B_in["x"],
        "B_wq": _B_in["wq"],
        "B_wk": _B_in["wk"],
        "B_wv": _B_in["wv"],
        "B_wo": _B_in["wo"],
    }

def reference(A_x, A_wqkv, A_wo, B_x, B_wq, B_wk, B_wv, B_wo):
    def _A_ref(x, wqkv, wo):
        B, S, D = x.shape
        H = 32; Dh = D // H
        qkv = (lambda x, *d: x.permute(*d))((x @ wqkv).reshape(B, S, 3, H, Dh), 2, 0, 3, 1, 4)
        q, k, v = qkv[0], qkv[1], qkv[2]
        phi = lambda t: torch.where(t > 0, t + 1, torch.exp(t)) + 1e-6
        pq, pk = phi(q), phi(k)
        kv = torch.einsum("bhsd,bhse->bhde", pk, v)
        z = torch.einsum("bhsd->bhd", pk)
        num = torch.einsum("bhqd,bhde->bhqe", pq, kv)
        den = torch.einsum("bhqd,bhd->bhq", pq, z)[..., None] + 1e-6
        o = num / den
        return (lambda x, *d: x.permute(*d))(o, 0, 2, 1, 3).reshape(B, S, D) @ wo

    def _B_ref(x, wq, wk, wv, wo):
        B, S, D = x.shape
        H = 32
        Dh = D // H
        q = (lambda x, *d: x.permute(*d))((x @ wq).reshape(B, S, H, Dh), 0, 2, 1, 3)
        k = x @ wk
        v = x @ wv
        scores = torch.einsum("bhqd,bkd->bhqk", q, k) * (Dh ** -0.5)
        p = _softmax(scores, axis=-1)
        o = torch.einsum("bhqk,bkd->bhqd", p, v)
        return (lambda x, *d: x.permute(*d))(o, 0, 2, 1, 3).reshape(B, S, H * Dh) @ wo

    x = A_x
    x = x + _A_ref(_layernorm(x, axis=-1), A_wqkv, A_wo)
    x = x + _B_ref(_layernorm(x, axis=-1), B_wq, B_wk, B_wv, B_wo)
    x = x + _A_ref(_layernorm(x, axis=-1), A_wqkv, A_wo)
    x = x + _B_ref(_layernorm(x, axis=-1), B_wq, B_wk, B_wv, B_wo)
    return x
```

```cpp
#include <hip/hip_runtime.h>
#include <hip/hip_bf16.h>
#include <stdint.h>

#define DI __device__ __forceinline__
typedef __attribute__((ext_vector_type(8))) short bf16x8;
typedef __attribute__((ext_vector_type(16))) float f32x16;
typedef __attribute__((ext_vector_type(4))) float f32x4;
typedef __attribute__((ext_vector_type(2))) float f32x2;
typedef __attribute__((ext_vector_type(4))) unsigned u32x4;
typedef __attribute__((ext_vector_type(2))) __bf16 bf2;

constexpr int S = 2048, D = 512;
constexpr float LN_EPS = 1e-5f;
constexpr float QSCALE = 0.25f * 1.4426950408889634f;
constexpr float LOG2E = 1.4426950408889634f;

DI unsigned pk2(float a, float b) { bf2 v = {(__bf16)a, (__bf16)b}; return __builtin_bit_cast(unsigned, v); }
DI float bflo(unsigned u) { return __uint_as_float(u << 16); }
DI float bfhi(unsigned u) { return __uint_as_float(u & 0xffff0000u); }
DI f32x16 mfma(u32x4 a, u32x4 b, f32x16 c) {
  return __builtin_amdgcn_mfma_f32_32x32x16_bf16(__builtin_bit_cast(bf16x8, a), __builtin_bit_cast(bf16x8, b), c, 0, 0, 0);
}
DI float dot2(unsigned a, unsigned b, float c) {
  return __builtin_amdgcn_fdot2_f32_bf16(__builtin_bit_cast(bf2, a), __builtin_bit_cast(bf2, b), c, false);
}
DI float xhalf_sum(float v) {
  auto r = __builtin_amdgcn_permlane32_swap(__float_as_uint(v), __float_as_uint(v), false, false);
  return __uint_as_float(r[0]) + __uint_as_float(r[1]);
}
DI float xhalf_max(float v) {
  auto r = __builtin_amdgcn_permlane32_swap(__float_as_uint(v), __float_as_uint(v), false, false);
  return fmaxf(__uint_as_float(r[0]), __uint_as_float(r[1]));
}
DI float phi(float t) {
  float e = __builtin_amdgcn_exp2f(t * LOG2E);
  return (t > 0.f ? t + 1.f : e) + 1e-6f;
}
DI u32x4 pack8(const f32x16& x, int s) {
  u32x4 p;
  p[0] = pk2(x[8 * s + 0], x[8 * s + 1]); p[1] = pk2(x[8 * s + 2], x[8 * s + 3]);
  p[2] = pk2(x[8 * s + 4], x[8 * s + 5]); p[3] = pk2(x[8 * s + 6], x[8 * s + 7]);
  return p;
}
constexpr unsigned ONES2 = 0x3f803f80u;

constexpr size_t IMG = (size_t)S * D * 2;
constexpr size_t OFF_X0 = 0, OFF_X1 = IMG, OFF_X2 = 2 * IMG, OFF_X3 = 3 * IMG;
constexpr size_t OFF_PQ = 4 * IMG, OFF_Q = 5 * IMG, OFF_O = 6 * IMG;
constexpr size_t OFF_WQKV = 7 * IMG;
constexpr size_t OFF_WOA = OFF_WQKV + 48 * 32768;
constexpr size_t OFF_WQ = OFF_WOA + 16 * 32768;
constexpr size_t OFF_WKV = OFF_WQ + 16 * 32768;
constexpr size_t OFF_WOB = OFF_WKV + 32768;
constexpr size_t OFF_K = OFF_WOB + 16 * 32768;
constexpr size_t OFF_V = OFF_K + 65536;
constexpr size_t OFF_KMAX = OFF_V + 65536;
constexpr size_t OFF_CSA = OFF_KMAX + 1024;
constexpr size_t OFF_CSB = OFF_CSA + 1536 * 4;
constexpr size_t OFF_KVZ = OFF_CSB + 4096;
constexpr int KVZ_N = 32 * 272;

__global__ __launch_bounds__(512) void k_prep(const uint16_t* __restrict__ x, const uint16_t* __restrict__ wqkv,
                                              const uint16_t* __restrict__ woa, const uint16_t* __restrict__ wq,
                                              const uint16_t* __restrict__ wk, const uint16_t* __restrict__ wv,
                                              const uint16_t* __restrict__ wob, char* __restrict__ ws) {
  __shared__ float red[16][32];
  const int tid = threadIdx.x, l = tid & 63, w = tid >> 6, r = l & 31, h = l >> 5;
  const int b = blockIdx.x;
  if (b < 97) {
    const uint16_t* src; int ld; u32x4* dst; float* cs = nullptr;
    if (b < 48)      { src = wqkv + b * 32 + r;        ld = 1536; dst = (u32x4*)(ws + OFF_WQKV) + b * 2048;        cs = (float*)(ws + OFF_CSA) + b * 32; }
    else if (b < 64) { src = woa + (b - 48) * 32 + r;  ld = 512;  dst = (u32x4*)(ws + OFF_WOA) + (b - 48) * 2048; }
    else if (b < 80) { src = wq + (b - 64) * 32 + r;   ld = 512;  dst = (u32x4*)(ws + OFF_WQ) + (b - 64) * 2048;  cs = (float*)(ws + OFF_CSB) + (b - 64) * 32; }
    else if (b < 81) { src = (r < 16 ? wk + r : wv + (r - 16)); ld = 16; dst = (u32x4*)(ws + OFF_WKV);             cs = (float*)(ws + OFF_CSB) + 512; }
    else             { src = wob + (b - 81) * 32 + r;  ld = 512;  dst = (u32x4*)(ws + OFF_WOB) + (b - 81) * 2048; }
    uint16_t e[4][8];
#pragma unroll
    for (int i = 0; i < 4; i++)
#pragma unroll
      for (int j = 0; j < 8; j++) e[i][j] = src[(size_t)(16 * (4 * w + i) + 8 * (j >> 2) + 4 * h + (j & 3)) * ld];
    float sum = 0.f;
#pragma unroll
    for (int i = 0; i < 4; i++) {
      u32x4 p;
#pragma unroll
      for (int j = 0; j < 4; j++) {
        p[j] = (unsigned)e[i][2 * j] | ((unsigned)e[i][2 * j + 1] << 16);
        sum += bflo(p[j]); sum += bfhi(p[j]);
      }
      dst[(4 * w + i) * 64 + l] = p;
    }
    if (cs) {
      red[w * 2 + h][r] = sum;
      __syncthreads();
      if (tid < 32) {
        float t = 0.f;
#pragma unroll
        for (int i = 0; i < 16; i++) t += red[i][tid];
        cs[tid] = t;
      }
    }
  } else if (b < 97 + 128) {
    const int bb = b - 97, tt = bb >> 1, half = bb & 1;
    u32x4* dst = (u32x4*)(ws + OFF_X0);
#pragma unroll
    for (int i = 0; i < 2; i++) {
      const int ks = half * 16 + 2 * w + i;
      u32x4 v = *(const u32x4*)(x + (size_t)(32 * tt + r) * D + 16 * ks + 8 * h);
      auto s0 = __builtin_amdgcn_permlane32_swap(v[0], v[2], false, false);
      auto s1 = __builtin_amdgcn_permlane32_swap(v[1], v[3], false, false);
      u32x4 o = {s0[0], s1[0], s0[1], s1[1]};
      dst[(tt * 32 + ks) * 64 + l] = o;
    }
  } else {
    float* z = (float*)(ws + OFF_KVZ);
    for (int i = tid; i < 2 * KVZ_N; i += 512) z[i] = 0.f;
  }
}

constexpr int A1_LDS = 96 * 1024 + 4 * 256 + 4 * 512 * 4 + 4 * 64 * 4;
__global__ __launch_bounds__(256, 1) void k_a1(const u32x4* __restrict__ ximg, const u32x4* __restrict__ wimg,
                                               const float* __restrict__ cs, u32x4* __restrict__ pqimg,
                                               float* __restrict__ kvz) {
  extern __shared__ __attribute__((aligned(16))) char smem[];
  u32x4* wl = (u32x4*)smem;
  float* st = (float*)(smem + 96 * 1024);
  float* pkv = (float*)(smem + 96 * 1024 + 1024);
  float* pz = pkv + 4 * 512;
  const int tid = threadIdx.x, l = tid & 63, r = l & 31, h = l >> 5;
  const int w = __builtin_amdgcn_readfirstlane(tid >> 6);
  const int b = blockIdx.x, xcd = b & 7, idx = b >> 3;
  const int rb = (xcd >> 1) * 4 + (idx & 3), hp = (xcd & 1) * 8 + (idx >> 2);
  const int tt = rb * 4 + w;

  u32x4 wr[24], xr[32];
#pragma unroll
  for (int c = 0; c < 4; c++) {
#pragma unroll
    for (int i = 0; i < 6; i++) {
      const int g = c * 24 + w * 6 + i, ks = g / 3, n3 = g - ks * 3;
      wr[c * 6 + i] = wimg[((n3 * 16 + hp) * 32 + ks) * 64 + l];
    }
#pragma unroll
    for (int i = 0; i < 8; i++) xr[c * 8 + i] = ximg[(tt * 32 + c * 8 + i) * 64 + l];
  }
  f32x4 csq[4];
#pragma unroll
  for (int i = 0; i < 4; i++) csq[i] = *(const f32x4*)(cs + hp * 32 + 8 * i + 4 * h);
  const float csk = cs[512 + hp * 32 + r], csv = cs[1024 + hp * 32 + r];

  f32x16 aq = {}, ak = {}, av = {};
  float sx = 0.f, sxx = 0.f;
#pragma unroll
  for (int c = 0; c < 4; c++) {
#pragma unroll
    for (int i = 0; i < 6; i++) wl[(c * 24 + w * 6 + i) * 64 + l] = wr[c * 6 + i];
    __syncthreads();
#pragma unroll
    for (int i = 0; i < 8; i++) {
      const int ks = c * 8 + i;
      const u32x4 xf = xr[ks];
      aq = mfma(wl[(ks * 3 + 0) * 64 + l], xf, aq);
      ak = mfma(xf, wl[(ks * 3 + 1) * 64 + l], ak);
      av = mfma(xf, wl[(ks * 3 + 2) * 64 + l], av);
#pragma unroll
      for (int j = 0; j < 4; j++) { sx = dot2(xf[j], ONES2, sx); sxx = dot2(xf[j], xf[j], sxx); }
    }
  }
  sx = xhalf_sum(sx); sxx = xhalf_sum(sxx);
  const float mean = sx * (1.f / D);
  const float var = fmaxf(sxx * (1.f / D) - mean * mean, 0.f);
  const float rstd = __builtin_amdgcn_rsqf(var + LN_EPS);
  const float mr = mean * rstd;
  if (h == 0) { st[w * 64 + r] = rstd; st[w * 64 + 32 + r] = mr; }
#pragma unroll
  for (int i = 0; i < 16; i++) aq[i] = phi(aq[i] * rstd - mr * csq[i >> 2][i & 3]);
  pqimg[(tt * 32 + 2 * hp + 0) * 64 + l] = pack8(aq, 0);
  pqimg[(tt * 32 + 2 * hp + 1) * 64 + l] = pack8(aq, 1);
  __builtin_amdgcn_fence(__ATOMIC_RELEASE, "wavefront");
  __builtin_amdgcn_wave_barrier();
  __builtin_amdgcn_fence(__ATOMIC_ACQUIRE, "wavefront");
#pragma unroll
  for (int g = 0; g < 4; g++) {
    const f32x4 rs = *(const f32x4*)(st + w * 64 + 8 * g + 4 * h);
    const f32x4 ms = *(const f32x4*)(st + w * 64 + 32 + 8 * g + 4 * h);
#pragma unroll
    for (int j = 0; j < 4; j++) {
      ak[4 * g + j] = phi(ak[4 * g + j] * rs[j] - ms[j] * csk);
      av[4 * g + j] = av[4 * g + j] * rs[j] - ms[j] * csv;
    }
  }
  const u32x4 pk0 = pack8(ak, 0), pk1 = pack8(ak, 1), v0 = pack8(av, 0), v1 = pack8(av, 1);
  f32x16 kv = {};
  kv = mfma(pk0, v0, kv);
  kv = mfma(pk1, v1, kv);
  float z = 0.f;
#pragma unroll
  for (int j = 0; j < 4; j++) { z = dot2(pk0[j], ONES2, z); z = dot2(pk1[j], ONES2, z); }
  pz[w * 64 + l] = z;
  {
    const bool hd = r >= 16;
    f32x4 a, c;
#pragma unroll
    for (int j = 0; j < 4; j++) { a[j] = hd ? kv[8 + j] : kv[j]; c[j] = hd ? kv[12 + j] : kv[4 + j]; }
    float* p = pkv + w * 512 + (r >> 4) * 256 + (h * 16 + (r & 15)) * 8;
    *(f32x4*)p = a; *(f32x4*)(p + 4) = c;
  }
  __syncthreads();
  float* dst = kvz + hp * 2 * 272;
#pragma unroll
  for (int i = 0; i < 2; i++) {
    const int e = tid + 256 * i;
    const float v = (pkv[e] + pkv[512 + e]) + (pkv[1024 + e] + pkv[1536 + e]);
    atomicAdd(dst + (e >> 8) * 272 + (e & 255), v);
  }
  if (tid < 32) {
    const int hd = tid >> 4, p = tid & 15, d = 8 * ((p & 7) >> 2) + 4 * (p >> 3) + (p & 3), c = hd * 16 + d;
    float v = 0.f;
#pragma unroll
    for (int i = 0; i < 4; i++) v += pz[i * 64 + c] + pz[i * 64 + 32 + c];
    atomicAdd(dst + hd * 272 + 256 + p, v);
  }
}

template <int MODE>
__global__ __launch_bounds__(256, 1) void k_oproj(const u32x4* __restrict__ aimg, const u32x4* __restrict__ wimg,
                                                  const float* __restrict__ kvz, const u32x4* __restrict__ ximg,
                                                  u32x4* __restrict__ outp) {
  extern __shared__ __attribute__((aligned(16))) char smem[];
  f32x4* red = (f32x4*)smem;
  const int tid = threadIdx.x, l = tid & 63, r = l & 31, h = l >> 5;
  const int w = __builtin_amdgcn_readfirstlane(tid >> 6);
  const int b = blockIdx.x, xcd = b & 7, idx = b >> 3;
  const int rb = xcd * 4 + (idx & 3), j = idx >> 2;
  f32x4 kz[8][2];
  u32x4 af[2][8], wf[2][8];
  if (MODE == 0) {
    const float* p = kvz + (8 * w) * 272 + (r < 16 ? (h * 16 + r) * 8 : 256 + h * 8);
#pragma unroll
    for (int i = 0; i < 8; i++) { kz[i][0] = *(const f32x4*)(p + i * 272); kz[i][1] = *(const f32x4*)(p + i * 272 + 4); }
  }
#pragma unroll
  for (int i = 0; i < 8; i++) {
    af[0][i] = aimg[((rb * 2 + 0) * 32 + 8 * w + i) * 64 + l];
    af[1][i] = aimg[((rb * 2 + 1) * 32 + 8 * w + i) * 64 + l];
    if (MODE != 0) {
      wf[0][i] = wimg[((j * 2 + 0) * 32 + 8 * w + i) * 64 + l];
      wf[1][i] = wimg[((j * 2 + 1) * 32 + 8 * w + i) * 64 + l];
    }
  }
  if (MODE == 0) {
#pragma unroll
    for (int i = 0; i < 8; i++) {
      wf[0][i] = wimg[((j * 2 + 0) * 32 + 8 * w + i) * 64 + l];
      wf[1][i] = wimg[((j * 2 + 1) * 32 + 8 * w + i) * 64 + l];
    }
  }
  const int ont = w & 1, ott = w >> 1;
  const int oidx = ((rb * 2 + ott) * 32 + (j * 2 + ont) * 2) * 64 + l;
  const u32x4 xr0 = ximg[oidx], xr1 = ximg[oidx + 64];
  __builtin_amdgcn_sched_barrier(0);

  if (MODE == 0) {
#pragma unroll
    for (int i = 0; i < 8; i++) {
      const u32x4 kf = {pk2(kz[i][0][0], kz[i][0][1]), pk2(kz[i][0][2], kz[i][0][3]), pk2(kz[i][1][0], kz[i][1][1]), pk2(kz[i][1][2], kz[i][1][3])};
#pragma unroll
      for (int t = 0; t < 2; t++) {
        f32x16 nd = mfma(kf, af[t][i], f32x16{});
        const float rc = __builtin_amdgcn_rcpf(nd[8] + 1e-6f);
#pragma unroll
        for (int e = 0; e < 8; e++) nd[e] *= rc;
        af[t][i] = pack8(nd, 0);
      }
    }
  }
  f32x16 acc[4] = {};
#pragma unroll
  for (int i = 0; i < 8; i++) {
    acc[0] = mfma(wf[0][i], af[0][i], acc[0]);
    acc[1] = mfma(wf[1][i], af[0][i], acc[1]);
    acc[2] = mfma(wf[0][i], af[1][i], acc[2]);
    acc[3] = mfma(wf[1][i], af[1][i], acc[3]);
  }
  f32x16 mine;
#pragma unroll
  for (int t = 0; t < 4; t++) {
    if (t == w) { mine = acc[t]; }
    else {
      const int slot = (w - t - 1) & 3;
#pragma unroll
      for (int g = 0; g < 4; g++) {
        f32x4 v = {acc[t][4 * g], acc[t][4 * g + 1], acc[t][4 * g + 2], acc[t][4 * g + 3]};
        red[((t * 3 + slot) * 4 + g) * 64 + l] = v;
      }
    }
  }
  __syncthreads();
#pragma unroll
  for (int s = 0; s < 3; s++)
#pragma unroll
    for (int g = 0; g < 4; g++) {
      const f32x4 v = red[((w * 3 + s) * 4 + g) * 64 + l];
#pragma unroll
      for (int e = 0; e < 4; e++) mine[4 * g + e] += v[e];
    }
#pragma unroll
  for (int e = 0; e < 4; e++) {
    mine[2 * e] += bflo(xr0[e]); mine[2 * e + 1] += bfhi(xr0[e]);
    mine[8 + 2 * e] += bflo(xr1[e]); mine[8 + 2 * e + 1] += bfhi(xr1[e]);
  }
  u32x4 o0 = pack8(mine, 0), o1 = pack8(mine, 1);
  if (MODE != 2) {
    outp[oidx] = o0; outp[oidx + 64] = o1;
  } else {
    auto a0 = __builtin_amdgcn_permlane32_swap(o0[0], o0[2], false, false);
    auto a1 = __builtin_amdgcn_permlane32_swap(o0[1], o0[3], false, false);
    auto b0 = __builtin_amdgcn_permlane32_swap(o1[0], o1[2], false, false);
    auto b1 = __builtin_amdgcn_permlane32_swap(o1[1], o1[3], false, false);
    u32x4 n0 = {a0[0], a1[0], a0[1], a1[1]}, n1 = {b0[0], b1[0], b0[1], b1[1]};
    u32x4* row = outp + (size_t)((rb * 2 + ott) * 32 + r) * 64 + (j * 2 + ont) * 4 + h;
    row[0] = n0; row[2] = n1;
  }
}

constexpr int B1_LDS = 5 * 3 * 4 * 1024 + 4 * 2 * 64 * 8 + 16 * 80;
__global__ __launch_bounds__(256, 1) void k_b1(const u32x4* __restrict__ ximg, const u32x4* __restrict__ wimg,
                                               const float* __restrict__ cs, u32x4* __restrict__ qimg,
                                               u32x4* __restrict__ kimg, u32x4* __restrict__ vimg,
                                               float* __restrict__ kmax2) {
  extern __shared__ __attribute__((aligned(16))) char smem[];
  f32x4* red = (f32x4*)smem;
  f32x2* st = (f32x2*)(smem + 5 * 3 * 4 * 1024);
  char* vt = smem + 5 * 3 * 4 * 1024 + 4 * 2 * 64 * 8;
  const int tid = threadIdx.x, l = tid & 63, r = l & 31, h = l >> 5;
  const int w = __builtin_amdgcn_readfirstlane(tid >> 6);
  const int b = blockIdx.x, xcd = b & 7, idx = b >> 3;
  const int rb = xcd * 4 + (idx & 3), j = idx >> 2;
  const bool extra = j < 2;
  u32x4 af[2][8], wf[2][8], wx[8];
#pragma unroll
  for (int i = 0; i < 8; i++) {
    af[0][i] = ximg[((rb * 2 + 0) * 32 + 8 * w + i) * 64 + l];
    af[1][i] = ximg[((rb * 2 + 1) * 32 + 8 * w + i) * 64 + l];
    wf[0][i] = wimg[((j * 2 + 0) * 32 + 8 * w + i) * 64 + l];
    wf[1][i] = wimg[((j * 2 + 1) * 32 + 8 * w + i) * 64 + l];
  }
  if (extra) {
#pragma unroll
    for (int i = 0; i < 8; i++) wx[i] = wimg[(16 * 32 + 8 * w + i) * 64 + l];
  }
  const int ont = w & 1, ott = w >> 1;
  f32x4 csq[4], csx[4];
#pragma unroll
  for (int i = 0; i < 4; i++) {
    csq[i] = *(const f32x4*)(cs + (j * 2 + ont) * 32 + 8 * i + 4 * h);
    csx[i] = *(const f32x4*)(cs + 512 + 8 * i + 4 * h);
  }
  __builtin_amdgcn_sched_barrier(0);
  f32x16 acc[4] = {};
  float sx[2] = {0.f, 0.f}, sxx[2] = {0.f, 0.f};
#pragma unroll
  for (int i = 0; i < 8; i++) {
    acc[0] = mfma(wf[0][i], af[0][i], acc[0]);
    acc[1] = mfma(wf[1][i], af[0][i], acc[1]);
    acc[2] = mfma(wf[0][i], af[1][i], acc[2]);
    acc[3] = mfma(wf[1][i], af[1][i], acc[3]);
#pragma unroll
    for (int t = 0; t < 2; t++)
#pragma unroll
      for (int e = 0; e < 4; e++) { sx[t] = dot2(af[t][i][e], ONES2, sx[t]); sxx[t] = dot2(af[t][i][e], af[t][i][e], sxx[t]); }
  }
  st[(w * 2 + 0) * 64 + l] = f32x2{sx[0], sxx[0]};
  st[(w * 2 + 1) * 64 + l] = f32x2{sx[1], sxx[1]};
  f32x16 accx = {};
  if (extra) {
#pragma unroll
    for (int i = 0; i < 8; i++) accx = mfma(wx[i], j == 0 ? af[0][i] : af[1][i], accx);
  }
  f32x16 mine;
#pragma unroll
  for (int t = 0; t < 4; t++) {
    if (t == w) { mine = acc[t]; }
    else {
      const int slot = (w - t - 1) & 3;
#pragma unroll
      for (int g = 0; g < 4; g++) {
        f32x4 v = {acc[t][4 * g], acc[t][4 * g + 1], acc[t][4 * g + 2], acc[t][4 * g + 3]};
        red[((t * 3 + slot) * 4 + g) * 64 + l] = v;
      }
    }
  }
  if (extra && w != 0) {
#pragma unroll
    for (int g = 0; g < 4; g++) {
      f32x4 v = {accx[4 * g], accx[4 * g + 1], accx[4 * g + 2], accx[4 * g + 3]};
      red[((4 * 3 + w - 1) * 4 + g) * 64 + l] = v;
    }
  }
  __syncthreads();
#pragma unroll
  for (int s = 0; s < 3; s++)
#pragma unroll
    for (int g = 0; g < 4; g++) {
      const f32x4 v = red[((w * 3 + s) * 4 + g) * 64 + l];
#pragma unroll
      for (int e = 0; e < 4; e++) mine[4 * g + e] += v[e];
    }
  {
    float a = 0.f, q = 0.f;
#pragma unroll
    for (int s = 0; s < 4; s++) {
      const f32x2 u = st[(s * 2 + ott) * 64 + r], v = st[(s * 2 + ott) * 64 + 32 + r];
      a += u[0] + v[0]; q += u[1] + v[1];
    }
    const float mean = a * (1.f / D);
    const float var = fmaxf(q * (1.f / D) - mean * mean, 0.f);
    const float rstd = __builtin_amdgcn_rsqf(var + LN_EPS) * QSCALE;
    const float mr = mean * rstd;
#pragma unroll
    for (int i = 0; i < 16; i++) mine[i] = mine[i] * rstd - mr * csq[i >> 2][i & 3];
    const int oidx = ((rb * 2 + ott) * 32 + (j * 2 + ont) * 2) * 64 + l;
    qimg[oidx] = pack8(mine, 0); qimg[oidx + 64] = pack8(mine, 1);
  }
  if (extra && w == 0) {
#pragma unroll
    for (int s = 0; s < 3; s++)
#pragma unroll
      for (int g = 0; g < 4; g++) {
        const f32x4 v = red[((4 * 3 + s) * 4 + g) * 64 + l];
#pragma unroll
        for (int e = 0; e < 4; e++) accx[4 * g + e] += v[e];
      }
    float a = 0.f, q = 0.f;
#pragma unroll
    for (int s = 0; s < 4; s++) {
      const f32x2 u = st[(s * 2 + j) * 64 + r], v = st[(s * 2 + j) * 64 + 32 + r];
      a += u[0] + v[0]; q += u[1] + v[1];
    }
    const float mean = a * (1.f / D);
    const float var = fmaxf(q * (1.f / D) - mean * mean, 0.f);
    const float rstd = __builtin_amdgcn_rsqf(var + LN_EPS);
    const float mr = mean * rstd;
#pragma unroll
    for (int i = 0; i < 16; i++) accx[i] = accx[i] * rstd - mr * csx[i >> 2][i & 3];
    const int tt = rb * 2 + j;
    const u32x4 kf = pack8(accx, 0), vf = pack8(accx, 1);
    kimg[tt * 64 + l] = kf;
    float n2 = 0.f;
#pragma unroll
    for (int e = 0; e < 4; e++) n2 = dot2(kf[e], kf[e], n2);
    n2 = xhalf_sum(n2);
#pragma unroll
    for (int o = 16; o >= 1; o >>= 1) n2 = fmaxf(n2, __shfl_xor(n2, o));
    if (l == 0) kmax2[tt] = n2;
#pragma unroll
    for (int e = 0; e < 8; e++) {
      const int dv = 8 * (e >> 2) + 4 * h + (e & 3);
      *(uint16_t*)(vt + dv * 80 + r * 2) = (uint16_t)(e & 1 ? vf[e >> 1] >> 16 : vf[e >> 1] & 0xffffu);
    }
    __builtin_amdgcn_fence(__ATOMIC_RELEASE, "wavefront");
    __builtin_amdgcn_wave_barrier();
    __builtin_amdgcn_fence(__ATOMIC_ACQUIRE, "wavefront");
    const int s = r >> 4, dv = r & 15;
    const uint2 lo = *(const uint2*)(vt + dv * 80 + (16 * s + 4 * h) * 2);
    const uint2 hi = *(const uint2*)(vt + dv * 80 + (16 * s + 8 + 4 * h) * 2);
    vimg[tt * 64 + s * 32 + h * 16 + dv] = u32x4{lo.x, lo.y, hi.x, hi.y};
  }
}

constexpr int AT_K = 0, AT_V = 65536, AT_ONE = 131072, AT_Q = AT_ONE + 4096, AT_KM = AT_Q + 8192, AT_LDS = AT_KM + 8 * 256;
DI void glds16(const void* g, unsigned lds_base) {
  unsigned sv;
  asm volatile("s_mov_b32 %0, m0\n\ts_mov_b32 m0, %2\n\ts_nop 0\n\tglobal_load_lds_dwordx4 %1, off\n\ts_mov_b32 m0, %0"
               : "=&s"(sv) : "v"(g), "s"(lds_base) : "memory");
}
DI void glds4(const void* g, unsigned lds_base) {
  unsigned sv;
  asm volatile("s_mov_b32 %0, m0\n\ts_mov_b32 m0, %2\n\ts_nop 0\n\tglobal_load_lds_dword %1, off\n\ts_mov_b32 m0, %0"
               : "=&s"(sv) : "v"(g), "s"(lds_base) : "memory");
}
#define WAIT_BAR(N) asm volatile("s_waitcnt vmcnt(" #N ") lgkmcnt(0)\n\ts_barrier" ::: "memory")
typedef __attribute__((address_space(3))) const u32x4* lds_frag_ptr;
#define EXP2(x) __builtin_amdgcn_exp2f(x)

template <int NT>
DI void attn_span(int t0, lds_frag_ptr kp, lds_frag_ptr vp, int vstep, const u32x4 qf, const f32x16& negm,
                  f32x16& sA, u32x4& kf, f32x16& o) {
#pragma unroll 1
  for (int t = t0; t < t0 + NT; t += 2) {
    f32x16 sB;
    {
      const u32x4 v0 = vp[0], v1 = vp[32];
      sB = mfma(kf, qf, negm);
      kf = kp[((t + 2) & 63) * 64];
#pragma unroll
      for (int i = 0; i < 8; i++) sA[i] = EXP2(sA[i]);
      o = mfma(v0, pack8(sA, 0), o);
#pragma unroll
      for (int i = 8; i < 16; i++) sA[i] = EXP2(sA[i]);
      o = mfma(v1, pack8(sA, 1), o);
    }
    {
      const u32x4 v0 = vp[64], v1 = vp[96];
      sA = mfma(kf, qf, negm);
      kf = kp[((t + 3) & 63) * 64];
#pragma unroll
      for (int i = 0; i < 8; i++) sB[i] = EXP2(sB[i]);
      o = mfma(v0, pack8(sB, 0), o);
#pragma unroll
      for (int i = 8; i < 16; i++) sB[i] = EXP2(sB[i]);
      o = mfma(v1, pack8(sB, 1), o);
    }
    vp += vstep;
  }
}

__global__ __launch_bounds__(512, 2) void k_attn(const u32x4* __restrict__ qimg, const u32x4* __restrict__ kimg,
                                                 const u32x4* __restrict__ vimg, const float* __restrict__ kmax2,
                                                 u32x4* __restrict__ oimg) {
  extern __shared__ __attribute__((aligned(16))) char smem[];
  const int tid = threadIdx.x, l = tid & 63, r = l & 31, h = l >> 5;
  const int w = __builtin_amdgcn_readfirstlane(tid >> 6);
  const int b = blockIdx.x, xcd = b & 7, idx = b >> 3;
  const int rb = xcd * 4 + (idx & 3), j = idx >> 2;
  const int tt = rb * 2 + (w >> 2), head = j * 4 + (w & 3);
  const unsigned lds0 = (unsigned)(uintptr_t)smem;
  const int fidx = (tt * 32 + head) * 64 + l;
  glds16(qimg + fidx, lds0 + AT_Q + w * 1024);
  glds4(kmax2 + l, lds0 + AT_KM + w * 256);
#pragma unroll
  for (int c = 0; c < 8; c++) {
    glds16(kimg + (c * 8 + w) * 64 + l, lds0 + AT_K + (c * 8 + w) * 1024);
    glds16(vimg + (c * 8 + w) * 64 + l, lds0 + AT_V + (c * 8 + w) * 1024);
  }
  if (tid < 256) ((u32x4*)(smem + AT_ONE))[tid] = u32x4{ONES2, ONES2, ONES2, ONES2};
  WAIT_BAR(14);
  const u32x4 qf = ((lds_frag_ptr)(smem + AT_Q))[w * 64 + l];
  float km = *(const float*)(smem + AT_KM + w * 256 + l * 4);
#pragma unroll
  for (int o = 32; o >= 1; o >>= 1) km = fmaxf(km, __shfl_xor(km, o));
  float q2 = 0.f;
#pragma unroll
  for (int e = 0; e < 4; e++) q2 = dot2(qf[e], qf[e], q2);
  q2 = xhalf_sum(q2);
  float m = __builtin_amdgcn_sqrtf(q2 * km) * 1.001f;

  const lds_frag_ptr kp = (lds_frag_ptr)(smem + AT_K) + l;
  const bool vl = r < 16;
  const lds_frag_ptr vp0 = vl ? (lds_frag_ptr)(smem + AT_V) + h * 16 + r : (lds_frag_ptr)(smem + AT_ONE) + (r & 15);
  const int vstep = vl ? 128 : 0;
  f32x16 o, negm, sA;
  u32x4 kf;
#pragma unroll
  for (int i = 0; i < 16; i++) { negm[i] = -m; o[i] = 0.f; }
  sA = mfma(kp[0], qf, negm);
  kf = kp[64];
  attn_span<6>(0, kp, vp0, vstep, qf, negm, sA, kf, o);
  WAIT_BAR(8);
  attn_span<16>(6, kp, vp0 + 3 * vstep, vstep, qf, negm, sA, kf, o);
  WAIT_BAR(0);
  attn_span<42>(22, kp, vp0 + 11 * vstep, vstep, qf, negm, sA, kf, o);
  const float lsum = o[8];
  if (__builtin_expect(__any(!(lsum >= 5.4e-20f && lsum < 3.0e38f)), 0)) {
    float mx = -INFINITY;
#pragma unroll 1
    for (int t = 0; t < 64; t++) {
      const f32x16 s = mfma(kp[t * 64], qf, f32x16{});
#pragma unroll
      for (int i = 0; i < 16; i++) mx = fmaxf(mx, s[i]);
    }
    mx = xhalf_max(mx);
#pragma unroll
    for (int i = 0; i < 16; i++) { negm[i] = -mx; o[i] = 0.f; }
    sA = mfma(kp[0], qf, negm);
    kf = kp[64];
    attn_span<64>(0, kp, vp0, vstep, qf, negm, sA, kf, o);
  }
  const float rc = __builtin_amdgcn_rcpf(o[8]);
#pragma unroll
  for (int e = 0; e < 8; e++) o[e] *= rc;
  oimg[fidx] = pack8(o, 0);
}

extern "C" void kernel_launch(void* const* d_in, const int* in_sizes, int n_in, void* d_out, int out_size,
                              void* d_ws, size_t ws_size, hipStream_t stream) {
  char* ws = (char*)d_ws;
  (void)hipFuncSetAttribute((const void*)k_a1, hipFuncAttributeMaxDynamicSharedMemorySize, A1_LDS);
  (void)hipFuncSetAttribute((const void*)k_attn, hipFuncAttributeMaxDynamicSharedMemorySize, AT_LDS);
  (void)hipFuncSetAttribute((const void*)k_b1, hipFuncAttributeMaxDynamicSharedMemorySize, B1_LDS);
  k_prep<<<97 + 128 + 1, 512, 0, stream>>>((const uint16_t*)d_in[0], (const uint16_t*)d_in[1], (const uint16_t*)d_in[2],
                                            (const uint16_t*)d_in[4], (const uint16_t*)d_in[5], (const uint16_t*)d_in[6],
                                            (const uint16_t*)d_in[7], ws);
  auto I = [&](size_t off) { return (u32x4*)(ws + off); };
  const float* csa = (const float*)(ws + OFF_CSA);
  const float* csb = (const float*)(ws + OFF_CSB);
  float* kmax2 = (float*)(ws + OFF_KMAX);
  for (int layer = 0; layer < 2; layer++) {
    const size_t xin = layer == 0 ? OFF_X0 : OFF_X2, xmid = layer == 0 ? OFF_X1 : OFF_X3;
    float* kvz = (float*)(ws + OFF_KVZ) + layer * KVZ_N;
    k_a1<<<256, 256, A1_LDS, stream>>>(I(xin), I(OFF_WQKV), csa, I(OFF_PQ), kvz);
    k_oproj<0><<<256, 256, 48 * 1024, stream>>>(I(OFF_PQ), I(OFF_WOA), kvz, I(xin), I(xmid));
    k_b1<<<256, 256, B1_LDS, stream>>>(I(xmid), I(OFF_WQ), csb, I(OFF_Q), I(OFF_K), I(OFF_V), kmax2);
    k_attn<<<256, 512, AT_LDS, stream>>>(I(OFF_Q), I(OFF_K), I(OFF_V), kmax2, I(OFF_O));
    if (layer == 0) k_oproj<1><<<256, 256, 48 * 1024, stream>>>(I(OFF_O), I(OFF_WOB), nullptr, I(xmid), I(OFF_X2));
    else            k_oproj<2><<<256, 256, 48 * 1024, stream>>>(I(OFF_O), I(OFF_WOB), nullptr, I(xmid), (u32x4*)d_out);
  }
}
```

```cpp
#include <hip/hip_runtime.h>
#include <hip/hip_bf16.h>
#include <stdint.h>

#define DI __device__ __forceinline__
typedef __attribute__((ext_vector_type(8))) short bf16x8;
typedef __attribute__((ext_vector_type(16))) float f32x16;
typedef __attribute__((ext_vector_type(8))) float f32x8;
typedef __attribute__((ext_vector_type(4))) float f32x4;
typedef __attribute__((ext_vector_type(2))) float f32x2;
typedef __attribute__((ext_vector_type(4))) unsigned u32x4;
typedef __attribute__((ext_vector_type(2))) unsigned u32x2;
typedef __attribute__((ext_vector_type(2))) __bf16 bf2;
typedef __attribute__((address_space(3))) const u32x4* lds_frag_ptr;

constexpr int S = 2048, D = 512;
constexpr float LN_EPS = 1e-5f;
constexpr float QSCALE = 0.25f * 1.4426950408889634f;
constexpr float LOG2E = 1.4426950408889634f;

DI unsigned pk2(float a, float b) { bf2 v = {(__bf16)a, (__bf16)b}; return __builtin_bit_cast(unsigned, v); }
DI float bflo(unsigned u) { return __uint_as_float(u << 16); }
DI float bfhi(unsigned u) { return __uint_as_float(u & 0xffff0000u); }
DI f32x16 mfma(u32x4 a, u32x4 b, f32x16 c) {
  return __builtin_amdgcn_mfma_f32_32x32x16_bf16(__builtin_bit_cast(bf16x8, a), __builtin_bit_cast(bf16x8, b), c, 0, 0, 0);
}
DI float dot2(unsigned a, unsigned b, float c) {
  return __builtin_amdgcn_fdot2_f32_bf16(__builtin_bit_cast(bf2, a), __builtin_bit_cast(bf2, b), c, false);
}
DI float xhalf_sum(float v) {
  auto r = __builtin_amdgcn_permlane32_swap(__float_as_uint(v), __float_as_uint(v), false, false);
  return __uint_as_float(r[0]) + __uint_as_float(r[1]);
}
DI float xhalf_max(float v) {
  auto r = __builtin_amdgcn_permlane32_swap(__float_as_uint(v), __float_as_uint(v), false, false);
  return fmaxf(__uint_as_float(r[0]), __uint_as_float(r[1]));
}
DI float phi1(float t1) {
  const float e = __builtin_amdgcn_fmed3f(__builtin_amdgcn_exp2f(t1 * LOG2E), 0.f, 1.f);
  return fmaxf(t1, 1e-6f) + e;
}
DI u32x4 pack8(const f32x16& x, int s) {
  u32x4 p;
  p[0] = pk2(x[8 * s + 0], x[8 * s + 1]); p[1] = pk2(x[8 * s + 2], x[8 * s + 3]);
  p[2] = pk2(x[8 * s + 4], x[8 * s + 5]); p[3] = pk2(x[8 * s + 6], x[8 * s + 7]);
  return p;
}
DI u32x4 pack8(const f32x8& x) { return u32x4{pk2(x[0], x[1]), pk2(x[2], x[3]), pk2(x[4], x[5]), pk2(x[6], x[7])}; }
constexpr unsigned ONES2 = 0x3f803f80u;
DI f32x2 fma2(f32x2 a, f32x2 b, f32x2 c) { return __builtin_elementwise_fma(a, b, c); }
DI f32x2 splat2(float a) { return f32x2{a, a}; }
template <class V> DI f32x2 get2(const V& v, int i) { return f32x2{v[i], v[i + 1]}; }
template <class V> DI void set2(V& v, int i, f32x2 x) { v[i] = x[0]; v[i + 1] = x[1]; }

constexpr int IMG = S * D * 2;
constexpr int OFF_FLAG = 0;
constexpr int FL_KV = 0, FL_SLAB = 512, FL_OA = 704, FL_X1 = 768, FL_OB = 832, FL_X2 = 896, FL_LAYER = 1024;
constexpr int FLAG_BYTES = 2 * FL_LAYER * 256;
constexpr int OFF_CSP = FLAG_BYTES;
constexpr int OFF_CSK = OFF_CSP + 16 * 2048 * 4;
constexpr int OFF_KV = OFF_CSK + 4096;
constexpr int OFF_SLAB = OFF_KV + 4 * 65536;
constexpr int SLAB_B = 544 * 4, SLAB_L = 256 * SLAB_B;
constexpr int OFF_RST = OFF_SLAB + 2 * SLAB_L;
constexpr int RST_L = 64 * 8192;
constexpr int OFF_WQKV = 4 * 1024 * 1024;
constexpr int OFF_WOA = OFF_WQKV + 48 * 32768;
constexpr int OFF_WQ = OFF_WOA + 16 * 32768;
constexpr int OFF_WKV = OFF_WQ + 16 * 32768;
constexpr int OFF_WOB = OFF_WKV + 32768;
constexpr int OFF_MKV = OFF_WOB + 16 * 32768;
constexpr int OFF_X = 8 * 1024 * 1024;
constexpr int OFF_OA = OFF_X + 2 * IMG, OFF_X1 = OFF_OA + 2 * IMG, OFF_OB = OFF_X1 + 2 * IMG;
constexpr int WS_USED = OFF_OB + 2 * IMG;
static_assert(OFF_RST + 4 * RST_L <= OFF_WQKV && OFF_MKV + 32768 <= OFF_X, "workspace map");

constexpr int P_BYTES = 32768 + 8 * 4608;
__global__ __launch_bounds__(512) void k_prep(const uint16_t* __restrict__ x, const uint16_t* __restrict__ wqkv,
                                              const uint16_t* __restrict__ woa, const uint16_t* __restrict__ wq,
                                              const uint16_t* __restrict__ wk, const uint16_t* __restrict__ wv,
                                              const uint16_t* __restrict__ wob, char* __restrict__ ws) {
  extern __shared__ __attribute__((aligned(16))) char tile[];
  const int tid = threadIdx.x, l = tid & 63, w = tid >> 6, r = l & 31, h = l >> 5;
  const int b = blockIdx.x < 16 ? blockIdx.x + 321 : blockIdx.x < 337 ? blockIdx.x - 16 : blockIdx.x;
  typedef short v4s __attribute__((ext_vector_type(4)));
  typedef __attribute__((address_space(3))) v4s* lds_v4s;
  if (b < 192) {
    const int band = b & 15, m = b >> 5, ch = (b >> 4) & 1;
    const uint16_t* src; int ld; u32x4* dst; float* cs = nullptr;
    if (m < 3)       { src = wqkv + m * 512; ld = 1536; dst = (u32x4*)(ws + OFF_WQKV) + m * 32768; cs = (float*)(ws + OFF_CSP) + band * 2048 + m * 512; }
    else if (m == 3) { src = woa; ld = 512; dst = (u32x4*)(ws + OFF_WOA); }
    else if (m == 4) { src = wq;  ld = 512; dst = (u32x4*)(ws + OFF_WQ);  cs = (float*)(ws + OFF_CSP) + band * 2048 + 1536; }
    else             { src = wob; ld = 512; dst = (u32x4*)(ws + OFF_WOB); }
    u32x4 v[2];
#pragma unroll
    for (int i = 0; i < 2; i++) v[i] = *(const u32x4*)(src + (size_t)(32 * band + 4 * w + 2 * i + h) * ld + ch * 256 + r * 8);
#pragma unroll
    for (int i = 0; i < 2; i++) *(u32x4*)(tile + (4 * w + 2 * i + h) * 576 + r * 16) = v[i];
    __syncthreads();
    const char* tp = tile + (4 * h + ((l & 15) >> 2)) * 576 + w * 64 + ((l >> 4) & 1) * 32 + (l & 3) * 8;
    float sum = 0.f;
#pragma unroll
    for (int k = 0; k < 2; k++) {
      const u32x2 lo = __builtin_bit_cast(u32x2, __builtin_amdgcn_ds_read_tr16_b64_v4i16((lds_v4s)(tp + k * 16 * 576)));
      const u32x2 hi = __builtin_bit_cast(u32x2, __builtin_amdgcn_ds_read_tr16_b64_v4i16((lds_v4s)(tp + (k * 16 + 8) * 576)));
      const u32x4 p = {lo[0], lo[1], hi[0], hi[1]};
      dst[((8 * ch + w) * 32 + 2 * band + k) * 64 + l] = p;
#pragma unroll
      for (int e = 0; e < 4; e++) { sum += bflo(p[e]); sum += bfhi(p[e]); }
    }
    sum = xhalf_sum(sum);
    if (cs && h == 0) cs[(8 * ch + w) * 32 + r] = sum;
  } else if (b < 193) {
    const int c8 = (l & 3) * 8;
    const uint16_t* src = (c8 < 16 ? wk + c8 : wv + (c8 - 16));
    u32x4 v[4];
#pragma unroll
    for (int i = 0; i < 4; i++) v[i] = *(const u32x4*)(src + (size_t)(64 * w + 16 * i + (l >> 2)) * 16);
#pragma unroll
    for (int i = 0; i < 4; i++) ((u32x4*)tile)[(4 * w + i) * 64 + l] = v[i];
    __builtin_amdgcn_fence(__ATOMIC_RELEASE, "wavefront");
    __builtin_amdgcn_wave_barrier();
    __builtin_amdgcn_fence(__ATOMIC_ACQUIRE, "wavefront");
    const char* tp = tile + (64 * w + 4 * h + ((l & 15) >> 2)) * 64 + ((l >> 4) & 1) * 32 + (l & 3) * 8;
#pragma unroll
    for (int i = 0; i < 2; i++) {
      float sum = 0.f;
#pragma unroll
      for (int k = 0; k < 2; k++) {
        const u32x2 lo = __builtin_bit_cast(u32x2, __builtin_amdgcn_ds_read_tr16_b64_v4i16((lds_v4s)(tp + (2 * i + k) * 1024)));
        const u32x2 hi = __builtin_bit_cast(u32x2, __builtin_amdgcn_ds_read_tr16_b64_v4i16((lds_v4s)(tp + (2 * i + k) * 1024 + 512)));
        const u32x4 p = {lo[0], lo[1], hi[0], hi[1]};
        ((u32x4*)(ws + OFF_WKV))[(4 * w + 2 * i + k) * 64 + l] = p;
#pragma unroll
        for (int e = 0; e < 4; e++) { sum += bflo(p[e]); sum += bfhi(p[e]); }
      }
      sum = xhalf_sum(sum);
      if (h == 0) ((float*)(ws + OFF_CSK))[(2 * w + i) * 32 + r] = sum;
    }
  } else if (b < 193 + 128) {
    const int tt = (b - 193) >> 1, ch = (b - 193) & 1;
    u32x4 v[2];
#pragma unroll
    for (int i = 0; i < 2; i++) v[i] = *(const u32x4*)(x + (size_t)(32 * tt + 4 * w + 2 * i + h) * D + ch * 256 + r * 8);
#pragma unroll
    for (int i = 0; i < 2; i++) *(u32x4*)(tile + (4 * w + 2 * i + h) * 528 + r * 16) = v[i];
    __syncthreads();
#pragma unroll
    for (int i = 0; i < 2; i++) {
      const int ks = 16 * ch + 2 * w + i;
      const u32x4 t = *(const u32x4*)(tile + r * 528 + (2 * w + i) * 32 + h * 16);
      auto s0 = __builtin_amdgcn_permlane32_swap(t[0], t[2], false, false);
      auto s1 = __builtin_amdgcn_permlane32_swap(t[1], t[3], false, false);
      const u32x4 o = {s0[0], s1[0], s0[1], s1[1]};
      ((u32x4*)(ws + OFF_X))[(tt * 32 + ks) * 64 + l] = o;
      float sx = 0.f, sxx = 0.f;
#pragma unroll
      for (int e = 0; e < 4; e++) { sx = dot2(o[e], ONES2, sx); sxx = dot2(o[e], o[e], sxx); }
      sx = xhalf_sum(sx); sxx = xhalf_sum(sxx);
      if (h == 0) *(f32x2*)(ws + OFF_RST + tt * 8192 + ks * 256 + r * 8) = f32x2{sx, sxx};
    }
  } else if (b < 321 + 16) {
    const int kt = b - 321;
    const uint16_t* src = ((l & 3) < 2 ? wk + (l & 3) * 8 : wv + ((l & 3) - 2) * 8);
    char* at = tile + 32768 + w * 4608;
    u32x4 v[4], a[4];
#pragma unroll
    for (int i = 0; i < 4; i++) a[i] = *(const u32x4*)(woa + (size_t)(32 * kt + 8 * i + (l >> 3)) * D + 64 * w + (l & 7) * 8);
#pragma unroll
    for (int i = 0; i < 4; i++) v[i] = *(const u32x4*)(src + (size_t)(64 * w + 16 * i + (l >> 2)) * 16);
#pragma unroll
    for (int i = 0; i < 4; i++) *(u32x4*)(at + (8 * i + (l >> 3)) * 144 + (l & 7) * 16) = a[i];
#pragma unroll
    for (int i = 0; i < 4; i++) ((u32x4*)tile)[(4 * w + i) * 64 + l] = v[i];
    __builtin_amdgcn_fence(__ATOMIC_RELEASE, "wavefront");
    __builtin_amdgcn_wave_barrier();
    __builtin_amdgcn_fence(__ATOMIC_ACQUIRE, "wavefront");
    const char* tp = tile + (64 * w + 8 * h + ((l & 15) >> 2)) * 64 + ((l >> 4) & 1) * 32 + (l & 3) * 8;
    f32x16 acc = {};
#pragma unroll
    for (int i = 0; i < 4; i++) {
      const u32x2 lo = __builtin_bit_cast(u32x2, __builtin_amdgcn_ds_read_tr16_b64_v4i16((lds_v4s)(tp + i * 1024)));
      const u32x2 hi = __builtin_bit_cast(u32x2, __builtin_amdgcn_ds_read_tr16_b64_v4i16((lds_v4s)(tp + i * 1024 + 256)));
      acc = mfma(*(const u32x4*)(at + r * 144 + i * 32 + h * 16), u32x4{lo[0], lo[1], hi[0], hi[1]}, acc);
    }
    f32x4* ex = (f32x4*)tile + l;
#pragma unroll
    for (int g = 0; g < 4; g++) ex[(w * 4 + g) * 64] = f32x4{acc[4 * g], acc[4 * g + 1], acc[4 * g + 2], acc[4 * g + 3]};
    __syncthreads();
    if (w < 4) {
      f32x4 u = ex[w * 64];
#pragma unroll
      for (int i = 1; i < 8; i++) u += ex[(i * 4 + w) * 64];
      *(u32x2*)(ws + OFF_MKV + (2 * kt + (w >> 1)) * 1024 + l * 16 + (w & 1) * 8) = u32x2{pk2(u[0], u[1]), pk2(u[2], u[3])};
    }
  } else {
    ((u32x4*)(ws + OFF_FLAG))[((b - 337) * 64 + (tid >> 3)) * 16 + (tid & 7)] = u32x4{0u, 0u, 0u, 0u};
  }
}

#define ATT_ASM_P2 \
  "ds_read_b128 v[244:247], %[ka] offset:0\n\t" \
  "ds_read_b128 v[252:255], %[va] offset:512\n\t" \
  "ds_read_b128 v[248:251], %[va] offset:0\n\t" \
  "v_mov_b32 v140, 0\n\t" \
  "v_mov_b32 v141, 0\n\t" \
  "v_mov_b32 v142, 0\n\t" \
  "v_mov_b32 v143, 0\n\t" \
  "v_mov_b32 v144, 0\n\t" \
  "v_mov_b32 v145, 0\n\t" \
  "v_mov_b32 v146, 0\n\t" \
  "v_mov_b32 v147, 0\n\t" \
  "v_mov_b32 v148, 0\n\t" \
  "v_mov_b32 v149, 0\n\t" \
  "v_mov_b32 v150, 0\n\t" \
  "v_mov_b32 v151, 0\n\t" \
  "v_mov_b32 v152, 0\n\t" \
  "v_mov_b32 v153, 0\n\t" \
  "v_mov_b32 v154, 0\n\t" \
  "v_mov_b32 v155, 0\n\t" \
  "v_mov_b32 v156, 0\n\t" \
  "v_mov_b32 v157, 0\n\t" \
  "v_mov_b32 v158, 0\n\t" \
  "v_mov_b32 v159, 0\n\t" \
  "v_mov_b32 v160, 0\n\t" \
  "v_mov_b32 v161, 0\n\t" \
  "v_mov_b32 v162, 0\n\t" \
  "v_mov_b32 v163, 0\n\t" \
  "v_mov_b32 v164, 0\n\t" \
  "v_mov_b32 v165, 0\n\t" \
  "v_mov_b32 v166, 0\n\t" \
  "v_mov_b32 v167, 0\n\t" \
  "v_mov_b32 v168, 0\n\t" \
  "v_mov_b32 v169, 0\n\t" \
  "v_mov_b32 v170, 0\n\t" \
  "v_mov_b32 v171, 0\n\t" \
  "v_mov_b32 v232, 0\n\t" \
  "v_mov_b32 v233, 0\n\t" \
  "v_mov_b32 v234, 0\n\t" \
  "v_mov_b32 v235, 0\n\t" \
  "s_waitcnt lgkmcnt(2)\n\t" \
  "v_mfma_f32_32x32x16_bf16 v[188:203], v[244:247], v[236:239], v[172:187]\n\t" \
  "v_mfma_f32_32x32x16_bf16 v[204:219], v[244:247], v[240:243], v[172:187]\n\t" \
  "ds_read_b128 v[244:247], %[ka] offset:1024\n\t" \
  "s_waitcnt lgkmcnt(0)\n\t" \
  "s_nop 7\n\t" \
  "s_nop 3\n\t" \
  ""
#define ATT_ASM_A2 \
  "s_mov_b32 %[sv], m0\n\t" \
  "v_exp_f32 v220, v188\n\t" \
  "v_exp_f32 v221, v189\n\t" \
  "v_exp_f32 v222, v190\n\t" \
  "v_cvt_pk_bf16_f32 v228, v220, v221\n\t" \
  "v_exp_f32 v223, v191\n\t" \
  "v_mfma_f32_32x32x16_bf16 v[156:171], v[252:255], v[232:235], v[156:171]\n\t" \
  "ds_read_b128 v[252:255], %[va] offset:512\n\t" \
  "v_exp_f32 v224, v192\n\t" \
  "v_cvt_pk_bf16_f32 v229, v222, v223\n\t" \
  "v_exp_f32 v225, v193\n\t" \
  "v_exp_f32 v226, v194\n\t" \
  "s_add_u32 %[t], %[rot], 1\n\t" \
  "s_and_b32 %[t], %[t], 7\n\t" \
  "s_lshl_b32 %[t], %[t], 13\n\t" \
  "s_add_u32 %[t], %[t], %[kb]\n\t" \
  "s_add_u32 m0, %[lk], 8192\n\t" \
  "s_nop 0\n\t" \
  "buffer_load_dwordx4 %[l16], %[rs], %[t] offen sc1 lds\n\t" \
  "v_cvt_pk_bf16_f32 v230, v224, v225\n\t" \
  "v_exp_f32 v227, v195\n\t" \
  "v_exp_f32 v220, v196\n\t" \
  "v_cvt_pk_bf16_f32 v231, v226, v227\n\t" \
  "v_exp_f32 v221, v197\n\t" \
  "v_exp_f32 v222, v198\n\t" \
  "s_waitcnt lgkmcnt(1)\n\t" \
  "v_mfma_f32_32x32x16_bf16 v[140:155], v[248:251], v[228:231], v[140:155]\n\t" \
  "v_cvt_pk_bf16_f32 v232, v220, v221\n\t" \
  "v_exp_f32 v223, v199\n\t" \
  "v_exp_f32 v224, v200\n\t" \
  "v_cvt_pk_bf16_f32 v233, v222, v223\n\t" \
  "s_add_u32 %[t], %[t], 0x10000\n\t" \
  "s_add_u32 m0, %[lk], 73728\n\t" \
  "s_nop 0\n\t" \
  "buffer_load_dwordx4 %[l16], %[rs], %[t] offen sc1 lds\n\t" \
  "v_exp_f32 v225, v201\n\t" \
  "v_exp_f32 v226, v202\n\t" \
  "v_cvt_pk_bf16_f32 v234, v224, v225\n\t" \
  "v_exp_f32 v227, v203\n\t" \
  "s_waitcnt lgkmcnt(0)\n\t" \
  "v_mfma_f32_32x32x16_bf16 v[188:203], v[244:247], v[236:239], v[172:187]\n\t" \
  "v_cvt_pk_bf16_f32 v235, v226, v227\n\t" \
  "v_exp_f32 v220, v204\n\t" \
  "v_exp_f32 v221, v205\n\t" \
  "v_exp_f32 v222, v206\n\t" \
  "v_cvt_pk_bf16_f32 v228, v220, v221\n\t" \
  "v_exp_f32 v223, v207\n\t" \
  "v_mfma_f32_32x32x16_bf16 v[140:155], v[252:255], v[232:235], v[140:155]\n\t" \
  "v_exp_f32 v224, v208\n\t" \
  "v_cvt_pk_bf16_f32 v229, v222, v223\n\t" \
  "v_exp_f32 v225, v209\n\t" \
  "v_exp_f32 v226, v210\n\t" \
  "s_add_u32 %[t], %[rot], 5\n\t" \
  "s_and_b32 %[t], %[t], 7\n\t" \
  "s_lshl_b32 %[t], %[t], 13\n\t" \
  "s_add_u32 %[t], %[t], %[kb]\n\t" \
  "s_add_u32 m0, %[lk], 40960\n\t" \
  "s_nop 0\n\t" \
  "buffer_load_dwordx4 %[l16], %[rs], %[t] offen sc1 lds\n\t" \
  "v_cvt_pk_bf16_f32 v230, v224, v225\n\t" \
  "v_exp_f32 v227, v211\n\t" \
  "v_exp_f32 v220, v212\n\t" \
  "v_cvt_pk_bf16_f32 v231, v226, v227\n\t" \
  "v_exp_f32 v221, v213\n\t" \
  "v_exp_f32 v222, v214\n\t" \
  "v_mfma_f32_32x32x16_bf16 v[156:171], v[248:251], v[228:231], v[156:171]\n\t" \
  "ds_read_b128 v[248:251], %[va] offset:1024\n\t" \
  "v_cvt_pk_bf16_f32 v232, v220, v221\n\t" \
  "v_exp_f32 v223, v215\n\t" \
  "v_exp_f32 v224, v216\n\t" \
  "v_cvt_pk_bf16_f32 v233, v222, v223\n\t" \
  "s_add_u32 %[t], %[t], 0x10000\n\t" \
  "s_add_u32 m0, %[lk], 106496\n\t" \
  "s_nop 0\n\t" \
  "buffer_load_dwordx4 %[l16], %[rs], %[t] offen sc1 lds\n\t" \
  "v_exp_f32 v225, v217\n\t" \
  "v_exp_f32 v226, v218\n\t" \
  "v_cvt_pk_bf16_f32 v234, v224, v225\n\t" \
  "v_exp_f32 v227, v219\n\t" \
  "v_mfma_f32_32x32x16_bf16 v[204:219], v[244:247], v[240:243], v[172:187]\n\t" \
  "ds_read_b128 v[244:247], %[ka] offset:2048\n\t" \
  "v_cvt_pk_bf16_f32 v235, v226, v227\n\t" \
  "v_exp_f32 v220, v188\n\t" \
  "v_exp_f32 v221, v189\n\t" \
  "v_exp_f32 v222, v190\n\t" \
  "v_cvt_pk_bf16_f32 v228, v220, v221\n\t" \
  "v_exp_f32 v223, v191\n\t" \
  "v_mfma_f32_32x32x16_bf16 v[156:171], v[252:255], v[232:235], v[156:171]\n\t" \
  "ds_read_b128 v[252:255], %[va] offset:1536\n\t" \
  "v_exp_f32 v224, v192\n\t" \
  "v_cvt_pk_bf16_f32 v229, v222, v223\n\t" \
  "v_exp_f32 v225, v193\n\t" \
  "v_exp_f32 v226, v194\n\t" \
  "s_add_u32 %[t], %[rot], 2\n\t" \
  "s_and_b32 %[t], %[t], 7\n\t" \
  "s_lshl_b32 %[t], %[t], 13\n\t" \
  "s_add_u32 %[t], %[t], %[kb]\n\t" \
  "s_add_u32 m0, %[lk], 16384\n\t" \
  "s_nop 0\n\t" \
  "buffer_load_dwordx4 %[l16], %[rs], %[t] offen sc1 lds\n\t" \
  "v_cvt_pk_bf16_f32 v230, v224, v225\n\t" \
  "v_exp_f32 v227, v195\n\t" \
  "v_exp_f32 v220, v196\n\t" \
  "v_cvt_pk_bf16_f32 v231, v226, v227\n\t" \
  "v_exp_f32 v221, v197\n\t" \
  "v_exp_f32 v222, v198\n\t" \
  "s_waitcnt lgkmcnt(1)\n\t" \
  "v_mfma_f32_32x32x16_bf16 v[140:155], v[248:251], v[228:231], v[140:155]\n\t" \
  "v_cvt_pk_bf16_f32 v232, v220, v221\n\t" \
  "v_exp_f32 v223, v199\n\t" \
  "v_exp_f32 v224, v200\n\t" \
  "v_cvt_pk_bf16_f32 v233, v222, v223\n\t" \
  "s_add_u32 %[t], %[t], 0x10000\n\t" \
  "s_add_u32 m0, %[lk], 81920\n\t" \
  "s_nop 0\n\t" \
  "buffer_load_dwordx4 %[l16], %[rs], %[t] offen sc1 lds\n\t" \
  "v_exp_f32 v225, v201\n\t" \
  "v_exp_f32 v226, v202\n\t" \
  "v_cvt_pk_bf16_f32 v234, v224, v225\n\t" \
  "v_exp_f32 v227, v203\n\t" \
  "s_waitcnt lgkmcnt(0)\n\t" \
  "v_mfma_f32_32x32x16_bf16 v[188:203], v[244:247], v[236:239], v[172:187]\n\t" \
  "v_cvt_pk_bf16_f32 v235, v226, v227\n\t" \
  "v_exp_f32 v220, v204\n\t" \
  "v_exp_f32 v221, v205\n\t" \
  "v_exp_f32 v222, v206\n\t" \
  "v_cvt_pk_bf16_f32 v228, v220, v221\n\t" \
  "v_exp_f32 v223, v207\n\t" \
  "v_mfma_f32_32x32x16_bf16 v[140:155], v[252:255], v[232:235], v[140:155]\n\t" \
  "v_exp_f32 v224, v208\n\t" \
  "v_cvt_pk_bf16_f32 v229, v222, v223\n\t" \
  "v_exp_f32 v225, v209\n\t" \
  "v_exp_f32 v226, v210\n\t" \
  "s_add_u32 %[t], %[rot], 6\n\t" \
  "s_and_b32 %[t], %[t], 7\n\t" \
  "s_lshl_b32 %[t], %[t], 13\n\t" \
  "s_add_u32 %[t], %[t], %[kb]\n\t" \
  "s_add_u32 m0, %[lk], 49152\n\t" \
  "s_nop 0\n\t" \
  "buffer_load_dwordx4 %[l16], %[rs], %[t] offen sc1 lds\n\t" \
  "v_cvt_pk_bf16_f32 v230, v224, v225\n\t" \
  "v_exp_f32 v227, v211\n\t" \
  "v_exp_f32 v220, v212\n\t" \
  "v_cvt_pk_bf16_f32 v231, v226, v227\n\t" \
  "v_exp_f32 v221, v213\n\t" \
  "v_exp_f32 v222, v214\n\t" \
  "v_mfma_f32_32x32x16_bf16 v[156:171], v[248:251], v[228:231], v[156:171]\n\t" \
  "ds_read_b128 v[248:251], %[va] offset:2048\n\t" \
  "v_cvt_pk_bf16_f32 v232, v220, v221\n\t" \
  "v_exp_f32 v223, v215\n\t" \
  "v_exp_f32 v224, v216\n\t" \
  "v_cvt_pk_bf16_f32 v233, v222, v223\n\t" \
  "s_add_u32 %[t], %[t], 0x10000\n\t" \
  "s_add_u32 m0, %[lk], 114688\n\t" \
  "s_nop 0\n\t" \
  "buffer_load_dwordx4 %[l16], %[rs], %[t] offen sc1 lds\n\t" \
  "v_exp_f32 v225, v217\n\t" \
  "v_exp_f32 v226, v218\n\t" \
  "v_cvt_pk_bf16_f32 v234, v224, v225\n\t" \
  "v_exp_f32 v227, v219\n\t" \
  "v_mfma_f32_32x32x16_bf16 v[204:219], v[244:247], v[240:243], v[172:187]\n\t" \
  "ds_read_b128 v[244:247], %[ka] offset:3072\n\t" \
  "v_cvt_pk_bf16_f32 v235, v226, v227\n\t" \
  "v_add_u32 %[ka], 2048, %[ka]\n\t" \
  "v_add_u32 %[va], %[vs], %[va]\n\t" \
  "v_exp_f32 v220, v188\n\t" \
  "v_exp_f32 v221, v189\n\t" \
  "v_exp_f32 v222, v190\n\t" \
  "v_cvt_pk_bf16_f32 v228, v220, v221\n\t" \
  "v_exp_f32 v223, v191\n\t" \
  "v_mfma_f32_32x32x16_bf16 v[156:171], v[252:255], v[232:235], v[156:171]\n\t" \
  "ds_read_b128 v[252:255], %[va] offset:512\n\t" \
  "v_exp_f32 v224, v192\n\t" \
  "v_cvt_pk_bf16_f32 v229, v222, v223\n\t" \
  "v_exp_f32 v225, v193\n\t" \
  "v_exp_f32 v226, v194\n\t" \
  "s_add_u32 %[t], %[rot], 3\n\t" \
  "s_and_b32 %[t], %[t], 7\n\t" \
  "s_lshl_b32 %[t], %[t], 13\n\t" \
  "s_add_u32 %[t], %[t], %[kb]\n\t" \
  "s_add_u32 m0, %[lk], 24576\n\t" \
  "s_nop 0\n\t" \
  "buffer_load_dwordx4 %[l16], %[rs], %[t] offen sc1 lds\n\t" \
  "v_cvt_pk_bf16_f32 v230, v224, v225\n\t" \
  "v_exp_f32 v227, v195\n\t" \
  "v_exp_f32 v220, v196\n\t" \
  "v_cvt_pk_bf16_f32 v231, v226, v227\n\t" \
  "v_exp_f32 v221, v197\n\t" \
  "v_exp_f32 v222, v198\n\t" \
  "s_waitcnt lgkmcnt(1)\n\t" \
  "v_mfma_f32_32x32x16_bf16 v[140:155], v[248:251], v[228:231], v[140:155]\n\t" \
  "v_cvt_pk_bf16_f32 v232, v220, v221\n\t" \
  "v_exp_f32 v223, v199\n\t" \
  "v_exp_f32 v224, v200\n\t" \
  "v_cvt_pk_bf16_f32 v233, v222, v223\n\t" \
  "s_add_u32 %[t], %[t], 0x10000\n\t" \
  "s_add_u32 m0, %[lk], 90112\n\t" \
  "s_nop 0\n\t" \
  "buffer_load_dwordx4 %[l16], %[rs], %[t] offen sc1 lds\n\t" \
  "v_exp_f32 v225, v201\n\t" \
  "v_exp_f32 v226, v202\n\t" \
  "v_cvt_pk_bf16_f32 v234, v224, v225\n\t" \
  "v_exp_f32 v227, v203\n\t" \
  "s_waitcnt lgkmcnt(0)\n\t" \
  "v_mfma_f32_32x32x16_bf16 v[188:203], v[244:247], v[236:239], v[172:187]\n\t" \
  "v_cvt_pk_bf16_f32 v235, v226, v227\n\t" \
  "v_exp_f32 v220, v204\n\t" \
  "v_exp_f32 v221, v205\n\t" \
  "v_exp_f32 v222, v206\n\t" \
  "v_cvt_pk_bf16_f32 v228, v220, v221\n\t" \
  "v_exp_f32 v223, v207\n\t" \
  "v_mfma_f32_32x32x16_bf16 v[140:155], v[252:255], v[232:235], v[140:155]\n\t" \
  "v_exp_f32 v224, v208\n\t" \
  "v_cvt_pk_bf16_f32 v229, v222, v223\n\t" \
  "v_exp_f32 v225, v209\n\t" \
  "v_exp_f32 v226, v210\n\t" \
  "s_add_u32 %[t], %[rot], 7\n\t" \
  "s_and_b32 %[t], %[t], 7\n\t" \
  "s_lshl_b32 %[t], %[t], 13\n\t" \
  "s_add_u32 %[t], %[t], %[kb]\n\t" \
  "s_add_u32 m0, %[lk], 57344\n\t" \
  "s_nop 0\n\t" \
  "buffer_load_dwordx4 %[l16], %[rs], %[t] offen sc1 lds\n\t" \
  "v_cvt_pk_bf16_f32 v230, v224, v225\n\t" \
  "v_exp_f32 v227, v211\n\t" \
  "v_exp_f32 v220, v212\n\t" \
  "v_cvt_pk_bf16_f32 v231, v226, v227\n\t" \
  "v_exp_f32 v221, v213\n\t" \
  "v_exp_f32 v222, v214\n\t" \
  "v_mfma_f32_32x32x16_bf16 v[156:171], v[248:251], v[228:231], v[156:171]\n\t" \
  "ds_read_b128 v[248:251], %[va] offset:1024\n\t" \
  "v_cvt_pk_bf16_f32 v232, v220, v221\n\t" \
  "v_exp_f32 v223, v215\n\t" \
  "v_exp_f32 v224, v216\n\t" \
  "v_cvt_pk_bf16_f32 v233, v222, v223\n\t" \
  "s_add_u32 %[t], %[t], 0x10000\n\t" \
  "s_add_u32 m0, %[lk], 122880\n\t" \
  "s_nop 0\n\t" \
  "buffer_load_dwordx4 %[l16], %[rs], %[t] offen sc1 lds\n\t" \
  "v_exp_f32 v225, v217\n\t" \
  "v_exp_f32 v226, v218\n\t" \
  "v_cvt_pk_bf16_f32 v234, v224, v225\n\t" \
  "v_exp_f32 v227, v219\n\t" \
  "v_mfma_f32_32x32x16_bf16 v[204:219], v[244:247], v[240:243], v[172:187]\n\t" \
  "ds_read_b128 v[244:247], %[ka] offset:2048\n\t" \
  "v_cvt_pk_bf16_f32 v235, v226, v227\n\t" \
  "v_exp_f32 v220, v188\n\t" \
  "v_exp_f32 v221, v189\n\t" \
  "v_exp_f32 v222, v190\n\t" \
  "v_cvt_pk_bf16_f32 v228, v220, v221\n\t" \
  "v_exp_f32 v223, v191\n\t" \
  "v_mfma_f32_32x32x16_bf16 v[156:171], v[252:255], v[232:235], v[156:171]\n\t" \
  "ds_read_b128 v[252:255], %[va] offset:1536\n\t" \
  "v_exp_f32 v224, v192\n\t" \
  "v_cvt_pk_bf16_f32 v229, v222, v223\n\t" \
  "v_exp_f32 v225, v193\n\t" \
  "v_exp_f32 v226, v194\n\t" \
  "v_cvt_pk_bf16_f32 v230, v224, v225\n\t" \
  "v_exp_f32 v227, v195\n\t" \
  "v_exp_f32 v220, v196\n\t" \
  "v_cvt_pk_bf16_f32 v231, v226, v227\n\t" \
  "v_exp_f32 v221, v197\n\t" \
  "v_exp_f32 v222, v198\n\t" \
  "s_waitcnt lgkmcnt(1)\n\t" \
  "v_mfma_f32_32x32x16_bf16 v[140:155], v[248:251], v[228:231], v[140:155]\n\t" \
  "v_cvt_pk_bf16_f32 v232, v220, v221\n\t" \
  "v_exp_f32 v223, v199\n\t" \
  "v_exp_f32 v224, v200\n\t" \
  "v_cvt_pk_bf16_f32 v233, v222, v223\n\t" \
  "v_exp_f32 v225, v201\n\t" \
  "v_exp_f32 v226, v202\n\t" \
  "v_cvt_pk_bf16_f32 v234, v224, v225\n\t" \
  "v_exp_f32 v227, v203\n\t" \
  "s_waitcnt lgkmcnt(0)\n\t" \
  "v_mfma_f32_32x32x16_bf16 v[188:203], v[244:247], v[236:239], v[172:187]\n\t" \
  "v_cvt_pk_bf16_f32 v235, v226, v227\n\t" \
  "v_exp_f32 v220, v204\n\t" \
  "v_exp_f32 v221, v205\n\t" \
  "v_exp_f32 v222, v206\n\t" \
  "v_cvt_pk_bf16_f32 v228, v220, v221\n\t" \
  "v_exp_f32 v223, v207\n\t" \
  "v_mfma_f32_32x32x16_bf16 v[140:155], v[252:255], v[232:235], v[140:155]\n\t" \
  "v_exp_f32 v224, v208\n\t" \
  "v_cvt_pk_bf16_f32 v229, v222, v223\n\t" \
  "v_exp_f32 v225, v209\n\t" \
  "v_exp_f32 v226, v210\n\t" \
  "v_cvt_pk_bf16_f32 v230, v224, v225\n\t" \
  "v_exp_f32 v227, v211\n\t" \
  "v_exp_f32 v220, v212\n\t" \
  "v_cvt_pk_bf16_f32 v231, v226, v227\n\t" \
  "v_exp_f32 v221, v213\n\t" \
  "v_exp_f32 v222, v214\n\t" \
  "v_mfma_f32_32x32x16_bf16 v[156:171], v[248:251], v[228:231], v[156:171]\n\t" \
  "ds_read_b128 v[248:251], %[va] offset:2048\n\t" \
  "v_cvt_pk_bf16_f32 v232, v220, v221\n\t" \
  "v_exp_f32 v223, v215\n\t" \
  "v_exp_f32 v224, v216\n\t" \
  "v_cvt_pk_bf16_f32 v233, v222, v223\n\t" \
  "v_exp_f32 v225, v217\n\t" \
  "v_exp_f32 v226, v218\n\t" \
  "v_cvt_pk_bf16_f32 v234, v224, v225\n\t" \
  "v_exp_f32 v227, v219\n\t" \
  "v_mfma_f32_32x32x16_bf16 v[204:219], v[244:247], v[240:243], v[172:187]\n\t" \
  "ds_read_b128 v[244:247], %[ka] offset:3072\n\t" \
  "v_cvt_pk_bf16_f32 v235, v226, v227\n\t" \
  "v_add_u32 %[ka], 2048, %[ka]\n\t" \
  "v_add_u32 %[va], %[vs], %[va]\n\t" \
  "s_setprio 0\n\t" \
  "v_exp_f32 v220, v188\n\t" \
  "v_exp_f32 v221, v189\n\t" \
  "v_exp_f32 v222, v190\n\t" \
  "v_cvt_pk_bf16_f32 v228, v220, v221\n\t" \
  "v_exp_f32 v223, v191\n\t" \
  "v_mfma_f32_32x32x16_bf16 v[156:171], v[252:255], v[232:235], v[156:171]\n\t" \
  "ds_read_b128 v[252:255], %[va] offset:512\n\t" \
  "v_exp_f32 v224, v192\n\t" \
  "v_cvt_pk_bf16_f32 v229, v222, v223\n\t" \
  "v_exp_f32 v225, v193\n\t" \
  "v_exp_f32 v226, v194\n\t" \
  "v_cvt_pk_bf16_f32 v230, v224, v225\n\t" \
  "v_exp_f32 v227, v195\n\t" \
  "v_exp_f32 v220, v196\n\t" \
  "v_cvt_pk_bf16_f32 v231, v226, v227\n\t" \
  "v_exp_f32 v221, v197\n\t" \
  "v_exp_f32 v222, v198\n\t" \
  "s_waitcnt lgkmcnt(1)\n\t" \
  "v_mfma_f32_32x32x16_bf16 v[140:155], v[248:251], v[228:231], v[140:155]\n\t" \
  "v_cvt_pk_bf16_f32 v232, v220, v221\n\t" \
  "v_exp_f32 v223, v199\n\t" \
  "v_exp_f32 v224, v200\n\t" \
  "v_cvt_pk_bf16_f32 v233, v222, v223\n\t" \
  "v_exp_f32 v225, v201\n\t" \
  "v_exp_f32 v226, v202\n\t" \
  "v_cvt_pk_bf16_f32 v234, v224, v225\n\t" \
  "v_exp_f32 v227, v203\n\t" \
  "s_waitcnt lgkmcnt(0)\n\t" \
  "v_mfma_f32_32x32x16_bf16 v[188:203], v[244:247], v[236:239], v[172:187]\n\t" \
  "v_cvt_pk_bf16_f32 v235, v226, v227\n\t" \
  "v_exp_f32 v220, v204\n\t" \
  "v_exp_f32 v221, v205\n\t" \
  "v_exp_f32 v222, v206\n\t" \
  "v_cvt_pk_bf16_f32 v228, v220, v221\n\t" \
  "v_exp_f32 v223, v207\n\t" \
  "v_mfma_f32_32x32x16_bf16 v[140:155], v[252:255], v[232:235], v[140:155]\n\t" \
  "v_exp_f32 v224, v208\n\t" \
  "v_cvt_pk_bf16_f32 v229, v222, v223\n\t" \
  "v_exp_f32 v225, v209\n\t" \
  "v_exp_f32 v226, v210\n\t" \
  "v_cvt_pk_bf16_f32 v230, v224, v225\n\t" \
  "v_exp_f32 v227, v211\n\t" \
  "v_exp_f32 v220, v212\n\t" \
  "v_cvt_pk_bf16_f32 v231, v226, v227\n\t" \
  "v_exp_f32 v221, v213\n\t" \
  "v_exp_f32 v222, v214\n\t" \
  "v_mfma_f32_32x32x16_bf16 v[156:171], v[248:251], v[228:231], v[156:171]\n\t" \
  "ds_read_b128 v[248:251], %[va] offset:1024\n\t" \
  "v_cvt_pk_bf16_f32 v232, v220, v221\n\t" \
  "v_exp_f32 v223, v215\n\t" \
  "v_exp_f32 v224, v216\n\t" \
  "v_cvt_pk_bf16_f32 v233, v222, v223\n\t" \
  "v_exp_f32 v225, v217\n\t" \
  "v_exp_f32 v226, v218\n\t" \
  "v_cvt_pk_bf16_f32 v234, v224, v225\n\t" \
  "v_exp_f32 v227, v219\n\t" \
  "v_mfma_f32_32x32x16_bf16 v[204:219], v[244:247], v[240:243], v[172:187]\n\t" \
  "ds_read_b128 v[244:247], %[ka] offset:2048\n\t" \
  "v_cvt_pk_bf16_f32 v235, v226, v227\n\t" \
  "v_exp_f32 v220, v188\n\t" \
  "v_exp_f32 v221, v189\n\t" \
  "v_exp_f32 v222, v190\n\t" \
  "v_cvt_pk_bf16_f32 v228, v220, v221\n\t" \
  "v_exp_f32 v223, v191\n\t" \
  "v_mfma_f32_32x32x16_bf16 v[156:171], v[252:255], v[232:235], v[156:171]\n\t" \
  "ds_read_b128 v[252:255], %[va] offset:1536\n\t" \
  "v_exp_f32 v224, v192\n\t" \
  "v_cvt_pk_bf16_f32 v229, v222, v223\n\t" \
  "v_exp_f32 v225, v193\n\t" \
  "v_exp_f32 v226, v194\n\t" \
  "v_cvt_pk_bf16_f32 v230, v224, v225\n\t" \
  "v_exp_f32 v227, v195\n\t" \
  "v_exp_f32 v220, v196\n\t" \
  "v_cvt_pk_bf16_f32 v231, v226, v227\n\t" \
  "v_exp_f32 v221, v197\n\t" \
  "v_exp_f32 v222, v198\n\t" \
  "s_waitcnt lgkmcnt(1)\n\t" \
  "v_mfma_f32_32x32x16_bf16 v[140:155], v[248:251], v[228:231], v[140:155]\n\t" \
  "v_cvt_pk_bf16_f32 v232, v220, v221\n\t" \
  "v_exp_f32 v223, v199\n\t" \
  "v_exp_f32 v224, v200\n\t" \
  "v_cvt_pk_bf16_f32 v233, v222, v223\n\t" \
  "v_exp_f32 v225, v201\n\t" \
  "v_exp_f32 v226, v202\n\t" \
  "v_cvt_pk_bf16_f32 v234, v224, v225\n\t" \
  "v_exp_f32 v227, v203\n\t" \
  "s_waitcnt lgkmcnt(0)\n\t" \
  "v_mfma_f32_32x32x16_bf16 v[188:203], v[244:247], v[236:239], v[172:187]\n\t" \
  "v_cvt_pk_bf16_f32 v235, v226, v227\n\t" \
  "v_exp_f32 v220, v204\n\t" \
  "v_exp_f32 v221, v205\n\t" \
  "v_exp_f32 v222, v206\n\t" \
  "v_cvt_pk_bf16_f32 v228, v220, v221\n\t" \
  "v_exp_f32 v223, v207\n\t" \
  "v_mfma_f32_32x32x16_bf16 v[140:155], v[252:255], v[232:235], v[140:155]\n\t" \
  "v_exp_f32 v224, v208\n\t" \
  "v_cvt_pk_bf16_f32 v229, v222, v223\n\t" \
  "v_exp_f32 v225, v209\n\t" \
  "v_exp_f32 v226, v210\n\t" \
  "v_cvt_pk_bf16_f32 v230, v224, v225\n\t" \
  "v_exp_f32 v227, v211\n\t" \
  "v_exp_f32 v220, v212\n\t" \
  "v_cvt_pk_bf16_f32 v231, v226, v227\n\t" \
  "v_exp_f32 v221, v213\n\t" \
  "v_exp_f32 v222, v214\n\t" \
  "v_mfma_f32_32x32x16_bf16 v[156:171], v[248:251], v[228:231], v[156:171]\n\t" \
  "ds_read_b128 v[248:251], %[va] offset:2048\n\t" \
  "v_cvt_pk_bf16_f32 v232, v220, v221\n\t" \
  "v_exp_f32 v223, v215\n\t" \
  "v_exp_f32 v224, v216\n\t" \
  "v_cvt_pk_bf16_f32 v233, v222, v223\n\t" \
  "v_exp_f32 v225, v217\n\t" \
  "v_exp_f32 v226, v218\n\t" \
  "v_cvt_pk_bf16_f32 v234, v224, v225\n\t" \
  "v_exp_f32 v227, v219\n\t" \
  "v_mfma_f32_32x32x16_bf16 v[204:219], v[244:247], v[240:243], v[172:187]\n\t" \
  "ds_read_b128 v[244:247], %[ka] offset:3072\n\t" \
  "v_cvt_pk_bf16_f32 v235, v226, v227\n\t" \
  "v_add_u32 %[ka], 2048, %[ka]\n\t" \
  "v_add_u32 %[va], %[vs], %[va]\n\t" \
  "s_mov_b32 m0, %[sv]\n\t" \
  "s_waitcnt vmcnt(0) lgkmcnt(0)\n\t" \
  "s_barrier\n\t" \
  "s_nop 7\n\t" \
  ""
#define ATT_ASM_B2 \
  "1:\n\t" \
  "v_exp_f32 v220, v188\n\t" \
  "v_exp_f32 v221, v189\n\t" \
  "v_exp_f32 v222, v190\n\t" \
  "v_cvt_pk_bf16_f32 v228, v220, v221\n\t" \
  "v_exp_f32 v223, v191\n\t" \
  "v_mfma_f32_32x32x16_bf16 v[156:171], v[252:255], v[232:235], v[156:171]\n\t" \
  "ds_read_b128 v[252:255], %[va] offset:512\n\t" \
  "v_exp_f32 v224, v192\n\t" \
  "v_cvt_pk_bf16_f32 v229, v222, v223\n\t" \
  "v_exp_f32 v225, v193\n\t" \
  "v_exp_f32 v226, v194\n\t" \
  "v_cvt_pk_bf16_f32 v230, v224, v225\n\t" \
  "v_exp_f32 v227, v195\n\t" \
  "v_exp_f32 v220, v196\n\t" \
  "v_cvt_pk_bf16_f32 v231, v226, v227\n\t" \
  "v_exp_f32 v221, v197\n\t" \
  "v_exp_f32 v222, v198\n\t" \
  "s_waitcnt lgkmcnt(1)\n\t" \
  "v_mfma_f32_32x32x16_bf16 v[140:155], v[248:251], v[228:231], v[140:155]\n\t" \
  "v_cvt_pk_bf16_f32 v232, v220, v221\n\t" \
  "v_exp_f32 v223, v199\n\t" \
  "v_exp_f32 v224, v200\n\t" \
  "v_cvt_pk_bf16_f32 v233, v222, v223\n\t" \
  "v_exp_f32 v225, v201\n\t" \
  "v_exp_f32 v226, v202\n\t" \
  "v_cvt_pk_bf16_f32 v234, v224, v225\n\t" \
  "v_exp_f32 v227, v203\n\t" \
  "s_waitcnt lgkmcnt(0)\n\t" \
  "v_mfma_f32_32x32x16_bf16 v[188:203], v[244:247], v[236:239], v[172:187]\n\t" \
  "v_cvt_pk_bf16_f32 v235, v226, v227\n\t" \
  "v_exp_f32 v220, v204\n\t" \
  "v_exp_f32 v221, v205\n\t" \
  "v_exp_f32 v222, v206\n\t" \
  "v_cvt_pk_bf16_f32 v228, v220, v221\n\t" \
  "v_exp_f32 v223, v207\n\t" \
  "v_mfma_f32_32x32x16_bf16 v[140:155], v[252:255], v[232:235], v[140:155]\n\t" \
  "v_exp_f32 v224, v208\n\t" \
  "v_cvt_pk_bf16_f32 v229, v222, v223\n\t" \
  "v_exp_f32 v225, v209\n\t" \
  "v_exp_f32 v226, v210\n\t" \
  "v_cvt_pk_bf16_f32 v230, v224, v225\n\t" \
  "v_exp_f32 v227, v211\n\t" \
  "v_exp_f32 v220, v212\n\t" \
  "v_cvt_pk_bf16_f32 v231, v226, v227\n\t" \
  "v_exp_f32 v221, v213\n\t" \
  "v_exp_f32 v222, v214\n\t" \
  "v_mfma_f32_32x32x16_bf16 v[156:171], v[248:251], v[228:231], v[156:171]\n\t" \
  "ds_read_b128 v[248:251], %[va] offset:1024\n\t" \
  "v_cvt_pk_bf16_f32 v232, v220, v221\n\t" \
  "v_exp_f32 v223, v215\n\t" \
  "v_exp_f32 v224, v216\n\t" \
  "v_cvt_pk_bf16_f32 v233, v222, v223\n\t" \
  "v_exp_f32 v225, v217\n\t" \
  "v_exp_f32 v226, v218\n\t" \
  "v_cvt_pk_bf16_f32 v234, v224, v225\n\t" \
  "v_exp_f32 v227, v219\n\t" \
  "v_mfma_f32_32x32x16_bf16 v[204:219], v[244:247], v[240:243], v[172:187]\n\t" \
  "ds_read_b128 v[244:247], %[ka] offset:2048\n\t" \
  "v_cvt_pk_bf16_f32 v235, v226, v227\n\t" \
  "v_exp_f32 v220, v188\n\t" \
  "v_exp_f32 v221, v189\n\t" \
  "v_exp_f32 v222, v190\n\t" \
  "v_cvt_pk_bf16_f32 v228, v220, v221\n\t" \
  "v_exp_f32 v223, v191\n\t" \
  "v_mfma_f32_32x32x16_bf16 v[156:171], v[252:255], v[232:235], v[156:171]\n\t" \
  "ds_read_b128 v[252:255], %[va] offset:1536\n\t" \
  "v_exp_f32 v224, v192\n\t" \
  "v_cvt_pk_bf16_f32 v229, v222, v223\n\t" \
  "v_exp_f32 v225, v193\n\t" \
  "v_exp_f32 v226, v194\n\t" \
  "v_cvt_pk_bf16_f32 v230, v224, v225\n\t" \
  "v_exp_f32 v227, v195\n\t" \
  "v_exp_f32 v220, v196\n\t" \
  "v_cvt_pk_bf16_f32 v231, v226, v227\n\t" \
  "v_exp_f32 v221, v197\n\t" \
  "v_exp_f32 v222, v198\n\t" \
  "s_waitcnt lgkmcnt(1)\n\t" \
  "v_mfma_f32_32x32x16_bf16 v[140:155], v[248:251], v[228:231], v[140:155]\n\t" \
  "v_cvt_pk_bf16_f32 v232, v220, v221\n\t" \
  "v_exp_f32 v223, v199\n\t" \
  "v_exp_f32 v224, v200\n\t" \
  "v_cvt_pk_bf16_f32 v233, v222, v223\n\t" \
  "v_exp_f32 v225, v201\n\t" \
  "v_exp_f32 v226, v202\n\t" \
  "v_cvt_pk_bf16_f32 v234, v224, v225\n\t" \
  "v_exp_f32 v227, v203\n\t" \
  "s_waitcnt lgkmcnt(0)\n\t" \
  "v_mfma_f32_32x32x16_bf16 v[188:203], v[244:247], v[236:239], v[172:187]\n\t" \
  "v_cvt_pk_bf16_f32 v235, v226, v227\n\t" \
  "v_exp_f32 v220, v204\n\t" \
  "v_exp_f32 v221, v205\n\t" \
  "v_exp_f32 v222, v206\n\t" \
  "v_cvt_pk_bf16_f32 v228, v220, v221\n\t" \
  "v_exp_f32 v223, v207\n\t" \
  "v_mfma_f32_32x32x16_bf16 v[140:155], v[252:255], v[232:235], v[140:155]\n\t" \
  "v_exp_f32 v224, v208\n\t" \
  "v_cvt_pk_bf16_f32 v229, v222, v223\n\t" \
  "v_exp_f32 v225, v209\n\t" \
  "v_exp_f32 v226, v210\n\t" \
  "v_cvt_pk_bf16_f32 v230, v224, v225\n\t" \
  "v_exp_f32 v227, v211\n\t" \
  "v_exp_f32 v220, v212\n\t" \
  "v_cvt_pk_bf16_f32 v231, v226, v227\n\t" \
  "v_exp_f32 v221, v213\n\t" \
  "v_exp_f32 v222, v214\n\t" \
  "v_mfma_f32_32x32x16_bf16 v[156:171], v[248:251], v[228:231], v[156:171]\n\t" \
  "ds_read_b128 v[248:251], %[va] offset:2048\n\t" \
  "v_cvt_pk_bf16_f32 v232, v220, v221\n\t" \
  "v_exp_f32 v223, v215\n\t" \
  "v_exp_f32 v224, v216\n\t" \
  "v_cvt_pk_bf16_f32 v233, v222, v223\n\t" \
  "v_exp_f32 v225, v217\n\t" \
  "v_exp_f32 v226, v218\n\t" \
  "v_cvt_pk_bf16_f32 v234, v224, v225\n\t" \
  "v_exp_f32 v227, v219\n\t" \
  "v_mfma_f32_32x32x16_bf16 v[204:219], v[244:247], v[240:243], v[172:187]\n\t" \
  "ds_read_b128 v[244:247], %[ka] offset:3072\n\t" \
  "v_cvt_pk_bf16_f32 v235, v226, v227\n\t" \
  "v_add_u32 %[ka], 2048, %[ka]\n\t" \
  "v_add_u32 %[va], %[vs], %[va]\n\t" \
  "s_sub_u32 %[n], %[n], 1\n\t" \
  "s_cmp_lg_u32 %[n], 0\n\t" \
  "s_cbranch_scc1 1b\n\t" \
  "s_waitcnt lgkmcnt(0)\n\t" \
  "s_nop 15\n\t" \
  ""
#define ATT_ASM_E2 \
  "v_mfma_f32_32x32x16_bf16 v[156:171], v[252:255], v[232:235], v[156:171]\n\t" \
  "s_nop 15\n\t" \
  ""
#define ATT_ASM_F \
  "ds_read_b128 v[244:247], %[ka]\n\t" \
  "ds_read_b128 v[248:251], %[ka] offset:1024\n\t" \
  "ds_read_b128 v[252:255], %[va] offset:512\n\t" \
  "v_mov_b32 v140, 0\n\t" \
  "v_mov_b32 v141, 0\n\t" \
  "v_mov_b32 v142, 0\n\t" \
  "v_mov_b32 v143, 0\n\t" \
  "v_mov_b32 v144, 0\n\t" \
  "v_mov_b32 v145, 0\n\t" \
  "v_mov_b32 v146, 0\n\t" \
  "v_mov_b32 v147, 0\n\t" \
  "v_mov_b32 v148, 0\n\t" \
  "v_mov_b32 v149, 0\n\t" \
  "v_mov_b32 v150, 0\n\t" \
  "v_mov_b32 v151, 0\n\t" \
  "v_mov_b32 v152, 0\n\t" \
  "v_mov_b32 v153, 0\n\t" \
  "v_mov_b32 v154, 0\n\t" \
  "v_mov_b32 v155, 0\n\t" \
  "v_mov_b32 v232, 0\n\t" \
  "v_mov_b32 v233, 0\n\t" \
  "v_mov_b32 v234, 0\n\t" \
  "v_mov_b32 v235, 0\n\t" \
  "s_waitcnt lgkmcnt(1)\n\t" \
  "v_mfma_f32_32x32x16_bf16 v[188:203], v[244:247], v[236:239], v[172:187]\n\t" \
  "v_mfma_f32_32x32x16_bf16 v[204:219], v[248:251], v[236:239], v[172:187]\n\t" \
  "s_waitcnt lgkmcnt(0)\n\t" \
  "ds_read_b128 v[248:251], %[va] offset:0\n\t" \
  "ds_read_b128 v[244:247], %[ka] offset:2048\n\t" \
  "s_nop 7\n\t" \
  "s_nop 3\n\t" \
  "s_mov_b32 %[n], 32\n\t" \
  "1:\n\t" \
  "v_exp_f32 v220, v188\n\t" \
  "v_exp_f32 v221, v189\n\t" \
  "v_exp_f32 v222, v190\n\t" \
  "v_cvt_pk_bf16_f32 v228, v220, v221\n\t" \
  "v_exp_f32 v223, v191\n\t" \
  "s_waitcnt lgkmcnt(2)\n\t" \
  "v_mfma_f32_32x32x16_bf16 v[140:155], v[252:255], v[232:235], v[140:155]\n\t" \
  "ds_read_b128 v[252:255], %[va] offset:512\n\t" \
  "v_exp_f32 v224, v192\n\t" \
  "v_cvt_pk_bf16_f32 v229, v222, v223\n\t" \
  "v_exp_f32 v225, v193\n\t" \
  "v_exp_f32 v226, v194\n\t" \
  "v_cvt_pk_bf16_f32 v230, v224, v225\n\t" \
  "v_exp_f32 v227, v195\n\t" \
  "v_exp_f32 v220, v196\n\t" \
  "v_cvt_pk_bf16_f32 v231, v226, v227\n\t" \
  "v_exp_f32 v221, v197\n\t" \
  "v_exp_f32 v222, v198\n\t" \
  "s_waitcnt lgkmcnt(2)\n\t" \
  "v_mfma_f32_32x32x16_bf16 v[140:155], v[248:251], v[228:231], v[140:155]\n\t" \
  "ds_read_b128 v[248:251], %[va] offset:1024\n\t" \
  "v_cvt_pk_bf16_f32 v232, v220, v221\n\t" \
  "v_exp_f32 v223, v199\n\t" \
  "v_exp_f32 v224, v200\n\t" \
  "v_cvt_pk_bf16_f32 v233, v222, v223\n\t" \
  "v_exp_f32 v225, v201\n\t" \
  "v_exp_f32 v226, v202\n\t" \
  "v_cvt_pk_bf16_f32 v234, v224, v225\n\t" \
  "v_exp_f32 v227, v203\n\t" \
  "s_waitcnt lgkmcnt(2)\n\t" \
  "v_mfma_f32_32x32x16_bf16 v[188:203], v[244:247], v[236:239], v[172:187]\n\t" \
  "ds_read_b128 v[244:247], %[ka] offset:3072\n\t" \
  "v_cvt_pk_bf16_f32 v235, v226, v227\n\t" \
  "v_exp_f32 v220, v204\n\t" \
  "v_exp_f32 v221, v205\n\t" \
  "v_exp_f32 v222, v206\n\t" \
  "v_cvt_pk_bf16_f32 v228, v220, v221\n\t" \
  "v_exp_f32 v223, v207\n\t" \
  "s_waitcnt lgkmcnt(2)\n\t" \
  "v_mfma_f32_32x32x16_bf16 v[140:155], v[252:255], v[232:235], v[140:155]\n\t" \
  "ds_read_b128 v[252:255], %[va] offset:1536\n\t" \
  "v_exp_f32 v224, v208\n\t" \
  "v_cvt_pk_bf16_f32 v229, v222, v223\n\t" \
  "v_exp_f32 v225, v209\n\t" \
  "v_exp_f32 v226, v210\n\t" \
  "v_cvt_pk_bf16_f32 v230, v224, v225\n\t" \
  "v_exp_f32 v227, v211\n\t" \
  "v_exp_f32 v220, v212\n\t" \
  "v_cvt_pk_bf16_f32 v231, v226, v227\n\t" \
  "v_exp_f32 v221, v213\n\t" \
  "v_exp_f32 v222, v214\n\t" \
  "s_waitcnt lgkmcnt(2)\n\t" \
  "v_mfma_f32_32x32x16_bf16 v[140:155], v[248:251], v[228:231], v[140:155]\n\t" \
  "ds_read_b128 v[248:251], %[va] offset:2048\n\t" \
  "v_cvt_pk_bf16_f32 v232, v220, v221\n\t" \
  "v_exp_f32 v223, v215\n\t" \
  "v_exp_f32 v224, v216\n\t" \
  "v_cvt_pk_bf16_f32 v233, v222, v223\n\t" \
  "v_exp_f32 v225, v217\n\t" \
  "v_exp_f32 v226, v218\n\t" \
  "v_cvt_pk_bf16_f32 v234, v224, v225\n\t" \
  "v_exp_f32 v227, v219\n\t" \
  "s_waitcnt lgkmcnt(2)\n\t" \
  "v_mfma_f32_32x32x16_bf16 v[204:219], v[244:247], v[236:239], v[172:187]\n\t" \
  "ds_read_b128 v[244:247], %[ka] offset:4096\n\t" \
  "v_cvt_pk_bf16_f32 v235, v226, v227\n\t" \
  "v_add_u32 %[ka], 2048, %[ka]\n\t" \
  "v_add_u32 %[va], %[vs], %[va]\n\t" \
  "s_sub_u32 %[n], %[n], 1\n\t" \
  "s_cmp_lg_u32 %[n], 0\n\t" \
  "s_cbranch_scc1 1b\n\t" \
  "s_waitcnt lgkmcnt(0)\n\t" \
  "v_mfma_f32_32x32x16_bf16 v[140:155], v[252:255], v[232:235], v[140:155]\n\t" \
  "s_nop 15\n\t" \
  ""
#define ATT_CLOB "v220", "v221", "v222", "v223", "v224", "v225", "v226", "v227", "v228", "v229", "v230", "v231"
#define ATT_CLOB_F "v188", "v189", "v190", "v191", "v192", "v193", "v194", "v195", "v196", "v197", "v198", "v199", "v200", "v201", "v202", "v203", "v204", "v205", "v206", "v207", "v208", "v209", "v210", "v211", "v212", "v213", "v214", "v215", "v216", "v217", "v218", "v219", "v220", "v221", "v222", "v223", "v224", "v225", "v226", "v227", "v228", "v229", "v230", "v231", "v232", "v233", "v234", "v235", "v244", "v245", "v246", "v247", "v248", "v249", "v250", "v251", "v252", "v253", "v254", "v255"
#define ATT_OA "{v[140:155]}"
#define ATT_OB "{v[156:171]}"
#define ATT_NM "{v[172:187]}"
#define ATT_SA "{v[188:203]}"
#define ATT_SB "{v[204:219]}"
#define ATT_P1 "{v[232:235]}"
#define ATT_QA "{v[236:239]}"
#define ATT_QB "{v[240:243]}"
#define ATT_K "{v[244:247]}"
#define ATT_V0 "{v[248:251]}"
#define ATT_V1 "{v[252:255]}"


struct AttState { f32x16 oa, ob, sa, sb; u32x4 p1, k, v0, v1; };
#define ATT_STATE(c) c ATT_OA(st.oa), c ATT_OB(st.ob), c ATT_SA(st.sa), c ATT_SB(st.sb), c ATT_P1(st.p1), c ATT_K(st.k), c ATT_V0(st.v0), c ATT_V1(st.v1)
#define ATT_IN ATT_NM(negm), ATT_QA(qa), ATT_QB(qb)
DI void attn_p(AttState& st, unsigned ka, unsigned va, const u32x4 qa, const u32x4 qb, const f32x16& negm) {
  asm volatile(ATT_ASM_P2 : ATT_STATE("=") : ATT_IN, [ka] "v"(ka), [va] "v"(va) : "memory");
}
DI void attn_a(AttState& st, unsigned& ka, unsigned& va, unsigned vs, const u32x4 qa, const u32x4 qb, const f32x16& negm,
               __amdgpu_buffer_rsrc_t rs, int l16, int kb, int rot, unsigned lk) {
  unsigned t, sv;
  asm volatile(ATT_ASM_A2
               : ATT_STATE("+"), [ka] "+v"(ka), [va] "+v"(va), [t] "=&s"(t), [sv] "=&s"(sv)
               : ATT_IN, [vs] "v"(vs), [rs] "s"(rs), [l16] "v"(l16), [kb] "s"(kb), [rot] "s"(rot), [lk] "s"(lk)
               : ATT_CLOB, "scc", "memory");
}
DI void attn_b(AttState& st, unsigned& ka, unsigned& va, unsigned vs, const u32x4 qa, const u32x4 qb, const f32x16& negm, int n) {
  asm volatile(ATT_ASM_B2
               : ATT_STATE("+"), [ka] "+v"(ka), [va] "+v"(va), [n] "+s"(n)
               : ATT_IN, [vs] "v"(vs)
               : ATT_CLOB, "scc", "memory");
}
DI void attn_e(AttState& st) { asm volatile(ATT_ASM_E2 : ATT_STATE("+") :: "memory"); }
DI f32x16 attn_full(unsigned ka, unsigned va, unsigned vs, const u32x4 qa, const f32x16& negm) {
  f32x16 o; int n;
  asm volatile(ATT_ASM_F : "=" ATT_OA(o), [ka] "+v"(ka), [va] "+v"(va), [n] "=&s"(n) : ATT_NM(negm), ATT_QA(qa), [vs] "v"(vs)
               : ATT_CLOB_F, "scc", "memory");
  return o;
}
DI void dma16_pl(__amdgpu_buffer_rsrc_t rs, int l16, int soff, unsigned lds_base) {
  unsigned sv;
  asm volatile("s_mov_b32 %0, m0\n\ts_mov_b32 m0, %3\n\ts_nop 0\n\tbuffer_load_dwordx4 %1, %4, %2 offen lds\n\ts_mov_b32 m0, %0"
               : "=&s"(sv) : "v"(l16), "s"(soff), "s"(lds_base), "s"(rs) : "memory");
}
DI void stage_w(__amdgpu_buffer_rsrc_t rs, int l16, int soff, unsigned lds_base, int i0, int i1) {
#pragma unroll 1
  for (int i = i0; i < i1; i += 2) {
#pragma unroll
    for (int u = 0; u < 2; u++) dma16_pl(rs, l16, soff + (i + u) * 1024, lds_base + (i + u) * 1024);
  }
}
DI void dma16_sc1(__amdgpu_buffer_rsrc_t rs, int l16, int soff, unsigned lds_base) {
  unsigned sv;
  asm volatile("s_mov_b32 %0, m0\n\ts_mov_b32 m0, %3\n\ts_nop 0\n\tbuffer_load_dwordx4 %1, %4, %2 offen sc1 lds\n\ts_mov_b32 m0, %0"
               : "=&s"(sv) : "v"(l16), "s"(soff), "s"(lds_base), "s"(rs) : "memory");
}

constexpr int L_W1 = 0, L_K = 0, L_V = 65536, L_RS = 96 * 1024, L_ONE = 144 * 1024, L_CS = L_ONE + 5120, L_KVZ = L_CS + 256 * 4, L_QX = L_KVZ + 1280, L_OX = 128 * 1024, L_BYTES = L_QX + 8192;
constexpr int L_ST = L_RS, L_PKV = L_RS + 1024, L_DUMP = L_PKV + 4 * 544 * 4;
constexpr int L_WT = 32768;
constexpr int L_XR = 0, L_ST4 = 16384, L_VT = 24576;
constexpr unsigned SPIN_CAP = 1u << 17;
#define DRAIN() asm volatile("s_waitcnt vmcnt(0)" ::: "memory")
#define LD_SC1(soff) __builtin_amdgcn_raw_buffer_load_b128(rs, l16, (soff), 16)
#define LD_PL(soff) __builtin_amdgcn_raw_buffer_load_b128(rs, l16, (soff), 0)
#define ST_SC1(v, soff) __builtin_amdgcn_raw_buffer_store_b128((v), rs, l16, (soff), 16)
#define OPAQUE(x) asm volatile("" : "+s"(x))

constexpr unsigned SENT = 0xffffffffu;
DI u32x4 sent4() { unsigned v; asm volatile("v_mov_b32 %0, -1" : "=v"(v)); return u32x4{v, v, v, v}; }
#define SENT4 sent4()
constexpr unsigned RETRY_CAP = 1u << 12;
DI void poll_flags(__amdgpu_buffer_rsrc_t rs, int line, int n, int l) {
  __builtin_amdgcn_s_setprio(3);
  const int i = l < n ? l : 0;
  const int off = (i >> 2) * 256 + (i & 3) * 4;
  line = __builtin_amdgcn_readfirstlane(line);
#pragma unroll 1
  for (unsigned spins = 0; spins < SPIN_CAP; ++spins) {
    asm volatile("" ::: "memory");
    if (__all(__builtin_amdgcn_raw_buffer_load_b32(rs, off, OFF_FLAG + line * 256, 16) != 0u)) break;
    __builtin_amdgcn_s_sleep(1);
  }
  asm volatile("" ::: "memory");
}
DI void set_flag(__amdgpu_buffer_rsrc_t rs, int line, int word) {
  __builtin_amdgcn_raw_buffer_store_b32(1u, rs, (line + (word >> 2)) * 256 + (word & 3) * 4, OFF_FLAG, 16);
}
DI unsigned umax3(unsigned a, unsigned b, unsigned c) { return max(max(a, b), c); }
DI unsigned chk4(unsigned m, const u32x4& v) { return umax3(umax3(m, v[0], v[1]), v[2], v[3]); }
DI u32x2 frag_stats(const u32x4& x) {
  float sx = 0.f, sxx = 0.f;
#pragma unroll
  for (int e = 0; e < 4; e++) { sx = dot2(x[e], ONES2, sx); sxx = dot2(x[e], x[e], sxx); }
  return u32x2{__float_as_uint(xhalf_sum(sx)), __float_as_uint(xhalf_sum(sxx))};
}
DI f32x4 sum_groups(f32x4 t) {
#pragma unroll
  for (int e = 0; e < 4; e++) {
    auto a = __builtin_amdgcn_permlane16_swap(__float_as_uint(t[e]), __float_as_uint(t[e]), false, false);
    t[e] = xhalf_sum(__uint_as_float(a[0]) + __uint_as_float(a[1]));
  }
  return t;
}
DI f32x8 reduce4(char* smem, const f32x16& a0, const f32x16& a1, int kq, int tt, int l) {
  f32x4* red = (f32x4*)(smem + L_RS) + tt * (4 * 3 * 2 * 64) + l;
  f32x8 mine;
#pragma unroll
  for (int f = 0; f < 4; f++) {
    const f32x16& a = f < 2 ? a0 : a1;
    const int o = (f & 1) * 8;
    if (f == kq) {
#pragma unroll
      for (int e = 0; e < 8; e++) mine[e] = a[o + e];
    } else {
      const int slot = (kq - f - 1) & 3;
      red[((f * 3 + slot) * 2 + 0) * 64] = f32x4{a[o], a[o + 1], a[o + 2], a[o + 3]};
      red[((f * 3 + slot) * 2 + 1) * 64] = f32x4{a[o + 4], a[o + 5], a[o + 6], a[o + 7]};
    }
  }
  __syncthreads();
#pragma unroll
  for (int s = 0; s < 3; s++) {
    const f32x4 u = red[((kq * 3 + s) * 2 + 0) * 64], v = red[((kq * 3 + s) * 2 + 1) * 64];
#pragma unroll
    for (int e = 0; e < 4; e++) { mine[e] += u[e]; mine[4 + e] += v[e]; }
  }
  return mine;
}

#define IDS \
    int l; asm volatile("v_mbcnt_lo_u32_b32 %0, -1, 0\n\tv_mbcnt_hi_u32_b32 %0, -1, %0" : "=v"(l)); \
    int w = w_; OPAQUE(w); const int r = l & 31, h = l >> 5, tid = w * 64 + l; \
    int b = blockIdx.x; OPAQUE(b); const int xcd = b & 7, idx = b >> 3; \
    const int rbA = (xcd >> 1) * 4 + (idx & 3), hp = (xcd & 1) * 8 + (idx >> 2); \
    const int rb = xcd * 4 + (idx & 3), j = idx >> 2; \
    const int kq = w & 3, tq = w >> 2; \
    const auto rs = __builtin_amdgcn_make_buffer_rsrc(ws, 0, WS_USED, 0x00020000); \
    const unsigned lds0 = (unsigned)(uintptr_t)smem; \
    const int l16 = l * 16; \
    const int myfrag = ((rb * 2 + tq) * 32 + 4 * j + kq) * 1024; \
    const int afrag0 = ((rb * 2 + tq) * 32 + 8 * kq) * 1024; \
    const int wfrag0 = (2 * j * 32 + 8 * kq) * 1024; \
    const int w1off = OFF_WQKV + (hp * 32 + 4 * w) * 1024; \
    const int oafrag = ((rbA * 4 + w) * 32 + 2 * hp) * 1024, slaboff = (hp * 16 + rbA) * SLAB_B + (w - 4) * 1024; \
  (void)0

__global__ __launch_bounds__(512, 2) void k_main(char* __restrict__ ws, u32x4* __restrict__ outp) {
  extern __shared__ __attribute__((aligned(16))) char smem[];
  const int w_ = __builtin_amdgcn_readfirstlane(threadIdx.x >> 6);
  {
    IDS;
    if (tid < 320) { const unsigned o1 = (tid & 11) == 0 ? ONES2 : 0u; ((u32x4*)(smem + L_ONE))[tid] = u32x4{o1, o1, o1, o1}; }
  }

#pragma unroll 1
  for (int L = 0; L < 2; L++) {
    const int fl = L * FL_LAYER;
    u32x4 wf[16], w1[12], xres;
    {
    IDS;
#define LD_WF(base, i) LD_PL((base) + wfrag0 + ((i) >> 3) * 32768 + ((i) & 7) * 1024)
#define RD_WT(i) ((lds_frag_ptr)(smem + L_WT))[(((i) >> 3) * 32 + 8 * kq + ((i) & 7)) * 64 + l]
    if (w < 4) {
      const int tt = rbA * 4 + w;
      constexpr int AH = 12;
      const int xo = OFF_X + L * IMG + tt * 32768, so = OFF_RST + L * RST_L + tt * 8192;
      const lds_frag_ptr wl = (lds_frag_ptr)(smem + L_W1) + l;
      float* st = (float*)(smem + L_ST) + w * 64;
      const float* cs = (const float*)(smem + L_CS);
      f32x16 aq, ak, av;
      u32x4 xr[32];
      f32x4 rsd[4], ms[4];
      float csk, csv, rstd1, mr1;
#pragma unroll 1
      for (unsigned tries = 0;; ++tries) {
        const bool stage = L == 0 && tries == 0;
        u32x4 sr[8], wb[3][2];
        unsigned mx = 0u;
        if (L > 0 && tries == 0) __syncthreads();
#pragma unroll
        for (int i = 0; i < AH; i++) xr[i] = LD_SC1(xo + i * 1024);
        __builtin_amdgcn_sched_barrier(0);
        if (stage) __syncthreads();
#pragma unroll
        for (int p = 0; p < 2; p++)
#pragma unroll
          for (int n = 0; n < 2; n++) wb[p][n] = wl[((1 + n) * 32 + p) * 64];
#pragma unroll
        for (int ks = 0; ks < 32; ks++) {
          if ((((ks & 7) == 6 && ks < 24) || ks == 30) && stage) __syncthreads();
          if (ks + 2 < 32) {
#pragma unroll
            for (int n = 0; n < 2; n++) wb[(ks + 2) % 3][n] = wl[((1 + n) * 32 + ks + 2) * 64];
          }
          if (ks + AH < 32) xr[ks + AH] = LD_SC1(xo + (ks + AH) * 1024);
          if (ks < 8) sr[ks] = LD_SC1(so + ks * 1024);
          __builtin_amdgcn_sched_barrier(0);
          const u32x4 xf = xr[ks];
          ak = mfma(xf, wb[ks % 3][0], ks ? ak : f32x16{});
          av = mfma(xf, wb[ks % 3][1], ks ? av : f32x16{});
          mx = chk4(mx, xf);
          asm volatile("" : "+v"(mx));
          if (ks == 19) {
            f32x4 t = __builtin_bit_cast(f32x4, sr[0]);
            mx = chk4(mx, sr[0]);
#pragma unroll
            for (int i = 1; i < 8; i++) { t += __builtin_bit_cast(f32x4, sr[i]); mx = chk4(mx, sr[i]); }
            t = sum_groups(t);
            const f32x2 mean = f32x2{t[0], t[2]} * (1.f / D);
            const f32x2 var = fma2(f32x2{t[1], t[3]}, splat2(1.f / D), -mean * mean);
            const f32x2 rsq = {__builtin_amdgcn_rsqf(fmaxf(var[0], 0.f) + LN_EPS), __builtin_amdgcn_rsqf(fmaxf(var[1], 0.f) + LN_EPS)};
            *(f32x2*)(st + (l & 47) * 2 - (l >> 5) * 32) = (l & 32) ? -mean * rsq : rsq;
            __builtin_amdgcn_fence(__ATOMIC_RELEASE, "wavefront");
            __builtin_amdgcn_wave_barrier();
            __builtin_amdgcn_fence(__ATOMIC_ACQUIRE, "wavefront");
          }
          if (ks == 30) {
            csk = cs[32 + r]; csv = cs[64 + r];
            rstd1 = st[r]; mr1 = st[32 + r];
#pragma unroll
            for (int g = 0; g < 4; g++) { rsd[g] = *(const f32x4*)(st + 8 * g + 4 * h); ms[g] = *(const f32x4*)(st + 32 + 8 * g + 4 * h); }
          }
          __builtin_amdgcn_sched_barrier(0);
        }
        if (!__any(mx == SENT) || tries >= RETRY_CAP) break;
      }
#pragma unroll
      for (int i = 0; i < 16; i += 2) {
        const f32x2 rs2 = get2(rsd[i >> 2], i & 3), ms2 = get2(ms[i >> 2], i & 3);
        set2(ak, i, fma2(get2(ak, i), rs2, fma2(ms2, splat2(csk), splat2(1e-6f))));
        set2(av, i, fma2(get2(av, i), rs2, ms2 * splat2(csv)));
      }
#pragma unroll
      for (int i = 0; i < 16; i++) ak[i] = phi1(ak[i]);
      const u32x4 pk0 = pack8(ak, 0), pk1 = pack8(ak, 1), v0 = pack8(av, 0), v1 = pack8(av, 1);
      f32x16 kv = {};
      kv = mfma(pk0, v0, kv);
      kv = mfma(pk1, v1, kv);
      float z = 0.f;
#pragma unroll
      for (int e = 0; e < 4; e++) { z = dot2(pk0[e], ONES2, z); z = dot2(pk1[e], ONES2, z); }
      z = xhalf_sum(z);
      float* pk = (float*)(smem + L_PKV) + w * 544;
      float* dump = (float*)(smem + L_DUMP) + l * 8;
      *(h == 0 ? pk + (r >> 4) * 272 + 256 + ((r >> 2) & 1) * 8 + (r & 3) + ((r >> 3) & 1) * 4 : dump) = z;
      float* p0 = r < 16 ? pk + (h * 16 + r) * 8 : dump;
      float* p1 = r < 16 ? dump : pk + 272 + (h * 16 + r - 16) * 8;
      *(f32x4*)p0 = f32x4{kv[0], kv[1], kv[2], kv[3]};   *(f32x4*)(p0 + 4) = f32x4{kv[4], kv[5], kv[6], kv[7]};
      *(f32x4*)p1 = f32x4{kv[8], kv[9], kv[10], kv[11]}; *(f32x4*)(p1 + 4) = f32x4{kv[12], kv[13], kv[14], kv[15]};
      __syncthreads();
      if (L == 0) {
        __builtin_amdgcn_s_sleep(8 / 2);
        ST_SC1(SENT4, OFF_OA + oafrag); ST_SC1(SENT4, OFF_OA + oafrag + 1024);
        ST_SC1(SENT4, OFF_X1 + myfrag);
        if (l < 16) ST_SC1(SENT4, OFF_RST + 2 * RST_L + (myfrag >> 2));
      } else {
        __builtin_amdgcn_s_sleep(8);
      }
      {
        u32x4 wq4[4];
#pragma unroll
        for (int p = 0; p < 3; p++) wq4[p] = wl[p * 64];
#pragma unroll
        for (int ks = 0; ks < 32; ks++) {
          if (ks + 3 < 32) wq4[(ks + 3) & 3] = wl[(ks + 3) * 64];
          __builtin_amdgcn_sched_barrier(0);
          aq = mfma(wq4[ks & 3], xr[ks], ks ? aq : f32x16{});
          __builtin_amdgcn_sched_barrier(0);
        }
      }
#pragma unroll
      for (int g = 0; g < 4; g++) {
        const f32x4 c4 = *(const f32x4*)(cs + 8 * g + 4 * h);
#pragma unroll
        for (int e = 0; e < 4; e += 2) set2(aq, 4 * g + e, fma2(get2(aq, 4 * g + e), splat2(rstd1), fma2(get2(c4, e), splat2(mr1), splat2(1e-6f))));
      }
#pragma unroll
      for (int i = 0; i < 16; i++) aq[i] = phi1(aq[i]);
      const u32x4 pq0 = pack8(aq, 0), pq1 = pack8(aq, 1);
      DRAIN();
      __syncthreads();
      const char* kz = smem + L_KVZ + (r < 16 ? (h * 16 + r) * 16 : 512 + h * 16);
      const u32x4 kf0 = *(const u32x4*)kz, kf1 = *(const u32x4*)(kz + 544);
      f32x16 n0 = mfma(kf0, pq0, f32x16{}), n1 = mfma(kf1, pq1, f32x16{});
      const float r0 = __builtin_amdgcn_rcpf(n0[8] + 1e-6f), r1 = __builtin_amdgcn_rcpf(n1[8] + 1e-6f);
#pragma unroll
      for (int e = 0; e < 8; e++) { n0[e] *= r0; n1[e] *= r1; }
      ST_SC1(pack8(n0, 0), OFF_OA + L * IMG + oafrag); ST_SC1(pack8(n1, 0), OFF_OA + L * IMG + oafrag + 1024);
    } else {
      if (L > 0) {
        if (w == 7) poll_flags(rs, FL_X2 + rbA * 4, 16, l);
        __syncthreads();
        __builtin_amdgcn_s_setprio(0);
      }
      if (L == 0) {
        const int t4 = tid - 256;
        const int co = t4 < 96 ? OFF_CSP + ((t4 >> 5) * 512 + hp * 32 + (t4 & 31)) * 4 : t4 < 160 ? OFF_CSP + (1536 + j * 64 + t4 - 96) * 4 : OFF_CSK + (t4 & 31) * 4;
        const int cstep = t4 < 160 ? 8192 : 128;
#pragma unroll
        for (int c = 0; c < 6; c++) {
#pragma unroll
          for (int i = 0; i < 4; i++) {
            const int ks = c < 4 ? 8 * c + 2 * (w - 4) + (i & 1) : 16 * (c - 4) + 4 * (w - 4) + i, n = c < 4 ? 1 + (i >> 1) : 0;
            dma16_pl(rs, l16, OFF_WQKV + ((n * 16 + hp) * 32 + ks) * 1024, lds0 + L_W1 + (n * 32 + ks) * 1024);
          }
          if (c > 1) asm volatile("s_waitcnt vmcnt(8)\n\ts_barrier" ::: "memory");
        }
        {
          float cp[16];
#pragma unroll
          for (int i = 0; i < 16; i++) cp[i] = __uint_as_float(__builtin_amdgcn_raw_buffer_load_b32(rs, co + i * cstep, 0, 0));
#pragma unroll
          for (int i = 8; i > 0; i >>= 1)
#pragma unroll
            for (int k = 0; k < i; k++) cp[k] += cp[k + i];
          ((float*)(smem + L_CS))[t4] = cp[0];
          __syncthreads();
        }
        if (tid < 256 + 136) ST_SC1(SENT4, OFF_SLAB + slaboff);
        ST_SC1(SENT4, OFF_X1 + myfrag);
        if (l < 16) ST_SC1(SENT4, OFF_RST + 2 * RST_L + (myfrag >> 2));
      }
      if (w == 7 && j < 2) {
        if (L > 0) __builtin_amdgcn_s_sleep(16);
        f32x16 ax;
#pragma unroll 1
        for (unsigned tries = 0;; ++tries) {
          unsigned mx = 0u;
#pragma unroll
          for (int c = 0; c < 2; c++) {
            u32x4 xf[16], wm[16];
#pragma unroll
            for (int i = 0; i < 16; i++) { xf[i] = LD_SC1(OFF_X + L * IMG + ((rb * 2 + j) * 32 + c * 16 + i) * 1024); wm[i] = LD_PL(OFF_WKV + (c * 16 + i) * 1024); }
            __builtin_amdgcn_sched_barrier(0);
#pragma unroll
            for (int i = 0; i < 16; i++) { ax = mfma(wm[i], xf[i], (c | i) ? ax : f32x16{}); mx = chk4(mx, xf[i]); }
          }
          if (!__any(mx == SENT) || tries >= RETRY_CAP) break;
        }
#pragma unroll
        for (int g = 0; g < 4; g++) ((f32x4*)(smem + L_QX))[g * 64 + l] = f32x4{ax[4 * g], ax[4 * g + 1], ax[4 * g + 2], ax[4 * g + 3]};
      }
      DRAIN();
      __syncthreads();
      if (w < 7) {
        __builtin_amdgcn_s_setprio(3);
        const bool on = tid < 256 + 136;
        const f32x4* p = (const f32x4*)(smem + L_PKV) + (tid - 256);
        const f32x4 v = (p[0] + p[136]) + (p[272] + p[408]);
        if (on) ST_SC1(__builtin_bit_cast(u32x4, v), OFF_SLAB + L * SLAB_L + slaboff);
        if (l == 0) set_flag(rs, fl + FL_SLAB + (hp * 3 + (w - 4)) * 4, rbA);
        poll_flags(rs, fl + FL_SLAB + (hp * 3 + (w - 4)) * 4, 16, l);
        f32x4 t;
        const int lo = on ? l16 : 0;
#pragma unroll 1
        for (unsigned tries = 0;; ++tries) {
          u32x4 sl[16];
          unsigned mx = 0u;
#pragma unroll
          for (int i = 0; i < 16; i++) sl[i] = __builtin_amdgcn_raw_buffer_load_b128(rs, lo, OFF_SLAB + L * SLAB_L + (hp * 16 + i) * SLAB_B + (w - 4) * 1024, 16);
          __builtin_amdgcn_sched_barrier(0);
          t = __builtin_bit_cast(f32x4, sl[0]); mx = chk4(mx, sl[0]);
#pragma unroll
          for (int i = 1; i < 16; i++) { t += __builtin_bit_cast(f32x4, sl[i]); mx = chk4(mx, sl[i]); }
          if (!__any(mx == SENT) || tries >= RETRY_CAP) break;
        }
        if (on) *(u32x2*)(smem + L_KVZ + (tid - 256) * 8) = u32x2{pk2(t[0], t[1]), pk2(t[2], t[3])};
        __builtin_amdgcn_s_setprio(0);
      } else {
        stage_w(rs, l16, OFF_WOA + j * 65536, lds0 + L_WT, 0, 64);
      }
      __syncthreads();
      if (w == 7) DRAIN();
    }
    __syncthreads();
    if (tid == 0) set_flag(rs, fl + FL_OA + rbA * 4, hp);
#pragma unroll
    for (int i = 0; i < 16; i++) wf[i] = RD_WT(i);
    __syncthreads();
    }
    {
    IDS;
    const bool extra = tq == j;
    if (w < 7) {
#pragma unroll
      for (int i = 0; i < 9; i++) dma16_pl(rs, l16, OFF_WQ + j * 65536 + (w * 9 + i) * 1024, lds0 + L_WT + (w * 9 + i) * 1024);
      if (w == 6) dma16_pl(rs, l16, OFF_WQ + j * 65536 + 63 * 1024, lds0 + L_WT + 63 * 1024);
    }
    xres = LD_SC1(OFF_X + L * IMG + myfrag);
    {
      f32x16 a0, a1;
      f32x4* xr = (f32x4*)(smem + L_XR) + l;
      const int ao = OFF_OA + L * IMG + afrag0;
      auto finish = [&]() {
        DRAIN();
        f32x8 m = reduce4(smem, a0, a1, kq, tq, l);
#pragma unroll
        for (int e = 0; e < 4; e++) { m[2 * e] += bflo(xres[e]); m[2 * e + 1] += bfhi(xres[e]); }
        xres = pack8(m);
        ST_SC1(xres, OFF_X1 + L * IMG + myfrag);
        const u32x2 fs = frag_stats(xres);
        if (h == 0) __builtin_amdgcn_raw_buffer_store_b64(fs, rs, l * 8, OFF_RST + (2 + L) * RST_L + (myfrag >> 2), 16);
        __syncthreads();
        if (tid == 0) set_flag(rs, fl + FL_X1 + rb * 2, j);
      };
      if (extra) {
        u32x4 wm[8];
#pragma unroll
        for (int i = 0; i < 8; i++) wm[i] = LD_PL(OFF_MKV + (8 * kq + i) * 1024);
        if (w == 7) poll_flags(rs, fl + FL_OA + (rb >> 1) * 4, 16, l);
        __syncthreads();
        __builtin_amdgcn_s_setprio(0);
        u32x4 af[8];
#pragma unroll 1
        for (unsigned tries = 0;; ++tries) {
          unsigned mx = 0u;
#pragma unroll
          for (int i = 0; i < 8; i++) af[i] = LD_SC1(ao + i * 1024);
          __builtin_amdgcn_sched_barrier(0);
#pragma unroll
          for (int i = 0; i < 8; i++) { a0 = mfma(wf[i], af[i], i ? a0 : f32x16{}); a1 = mfma(wf[8 + i], af[i], i ? a1 : f32x16{}); mx = chk4(mx, af[i]); asm volatile("" : "+v"(mx)); __builtin_amdgcn_sched_barrier(0); }
          if (!__any(mx == SENT) || tries >= RETRY_CAP) break;
        }
        finish();
        f32x16 ay;
#pragma unroll
        for (int i = 0; i < 8; i++) ay = mfma(wm[i], af[i], i ? ay : f32x16{});
#pragma unroll
        for (int g = 0; g < 4; g++) xr[(((g >> 1) * 4 + kq) * 2 + (g & 1)) * 64] = f32x4{ay[4 * g], ay[4 * g + 1], ay[4 * g + 2], ay[4 * g + 3]};
      } else {
        if (w == 7) poll_flags(rs, fl + FL_OA + (rb >> 1) * 4, 16, l);
        __syncthreads();
        __builtin_amdgcn_s_setprio(0);
#pragma unroll 1
        for (unsigned tries = 0;; ++tries) {
          u32x4 af[8];
          unsigned mx = 0u;
#pragma unroll
          for (int i = 0; i < 8; i++) af[i] = LD_SC1(ao + i * 1024);
          __builtin_amdgcn_sched_barrier(0);
#pragma unroll
          for (int i = 0; i < 8; i++) { a0 = mfma(wf[i], af[i], i ? a0 : f32x16{}); a1 = mfma(wf[8 + i], af[i], i ? a1 : f32x16{}); mx = chk4(mx, af[i]); asm volatile("" : "+v"(mx)); __builtin_amdgcn_sched_barrier(0); }
          if (!__any(mx == SENT) || tries >= RETRY_CAP) break;
        }
        finish();
      }
    }
    }
    {
    IDS;
    {
      f32x16 a0, a1;
      float rstd, mr;
      f32x4* st = (f32x4*)(smem + L_ST4);
      const float* csb = (const float*)(smem + L_CS);
      const int so = OFF_RST + (2 + L) * RST_L + (rb * 2 + tq) * 8192, ao = OFF_X1 + L * IMG + afrag0;
      if (tq == j && kq < 2) {
        const f32x4 cx0 = *(const f32x4*)(csb + 160 + kq * 16 + 4 * h), cx1 = *(const f32x4*)(csb + 160 + kq * 16 + 8 + 4 * h);
        const f32x4* xk = (const f32x4*)(smem + L_XR) + l + kq * 512;
        const f32x4* xq = (const f32x4*)(smem + L_QX) + l + kq * 128;
        __builtin_amdgcn_s_setprio(3);
        __syncthreads();
        f32x4 t0, t1;
        f32x4 t;
#pragma unroll 1
        for (unsigned tries = 0;; ++tries) {
          u32x4 sr[8];
          unsigned mx = 0u;
#pragma unroll
          for (int i = 0; i < 8; i++) sr[i] = LD_SC1(so + i * 1024);
          __builtin_amdgcn_sched_barrier(0);
          t0 = ((xk[0] + xk[128]) + (xk[256] + xk[384])) + xq[0]; t1 = ((xk[64] + xk[192]) + (xk[320] + xk[448])) + xq[64];
          asm volatile("" : "+v"(t0), "+v"(t1));
          __builtin_amdgcn_sched_barrier(0);
          t = __builtin_bit_cast(f32x4, sr[0]); mx = chk4(mx, sr[0]);
#pragma unroll
          for (int i = 1; i < 8; i++) { t += __builtin_bit_cast(f32x4, sr[i]); mx = chk4(mx, sr[i]); }
          if (!__any(mx == SENT) || tries >= RETRY_CAP) break;
        }
        t = sum_groups(t);
        const int src = (r >> 1) * 4;
        const float sa = __uint_as_float(__builtin_amdgcn_ds_bpermute(src, __float_as_uint(t[0]))), sb = __uint_as_float(__builtin_amdgcn_ds_bpermute(src, __float_as_uint(t[2])));
        const float qa = __uint_as_float(__builtin_amdgcn_ds_bpermute(src, __float_as_uint(t[1]))), qb = __uint_as_float(__builtin_amdgcn_ds_bpermute(src, __float_as_uint(t[3])));
        const float mean = ((r & 1) ? sb : sa) * (1.f / D);
        const float var = fmaxf(((r & 1) ? qb : qa) * (1.f / D) - mean * mean, 0.f);
        const float rk = __builtin_amdgcn_rsqf(var + LN_EPS), mk = -mean * rk;
        t0 = t0 * rk + cx0 * mk; t1 = t1 * rk + cx1 * mk;
        u32x4 f = {pk2(t0[0], t0[1]), pk2(t0[2], t0[3]), pk2(t1[0], t1[1]), pk2(t1[2], t1[3])};
        int off = l16;
        if (kq == 1) {
          char* vt = smem + L_VT;
#pragma unroll
          for (int e = 0; e < 8; e++)
            *(uint16_t*)(vt + (8 * (e >> 2) + 4 * h + (e & 3)) * 80 + r * 2) = (uint16_t)(e & 1 ? f[e >> 1] >> 16 : f[e >> 1] & 0xffffu);
          __builtin_amdgcn_fence(__ATOMIC_RELEASE, "wavefront");
          __builtin_amdgcn_wave_barrier();
          __builtin_amdgcn_fence(__ATOMIC_ACQUIRE, "wavefront");
          const int s = r >> 4, dv = r & 15;
          const u32x2 lo = *(const u32x2*)(vt + dv * 80 + (16 * s + 4 * h) * 2);
          const u32x2 hi = *(const u32x2*)(vt + dv * 80 + (16 * s + 8 + 4 * h) * 2);
          f = u32x4{lo[0], lo[1], hi[0], hi[1]};
          off = 65536 + (s * 32 + h * 16 + dv) * 16;
        }
        __builtin_amdgcn_raw_buffer_store_b128(f, rs, off, OFF_KV + L * 131072 + (rb * 2 + j) * 1024, 16);
      } else {
        if (w == 7) poll_flags(rs, fl + FL_X1 + rb * 2, 8, l);
        __syncthreads();
        __builtin_amdgcn_s_setprio(0);
        if (j < 2) __builtin_amdgcn_s_sleep(6);
      }
#pragma unroll 1
      for (unsigned tries = 0;; ++tries) {
        u32x4 af[8];
        unsigned mx = 0u;
        const u32x4 s0 = LD_SC1(so + kq * 2048), s1 = LD_SC1(so + kq * 2048 + 1024);
#pragma unroll
        for (int i = 0; i < 8; i++) af[i] = LD_SC1(ao + i * 1024);
        u32x4 wg[16];
#pragma unroll
        for (int i = 0; i < 16; i++) wg[i] = RD_WT(i);
        __builtin_amdgcn_sched_barrier(0);
        mx = chk4(chk4(mx, s0), s1);
        if (tq == j && kq < 2) {
          DRAIN();
          if (l < 16) set_flag(rs, fl + FL_KV + (l * 2 + kq) * 16, rb * 2 + j);
          __builtin_amdgcn_s_setprio(0);
        }
        const f32x4 t = sum_groups(__builtin_bit_cast(f32x4, s0) + __builtin_bit_cast(f32x4, s1));
        if (l < 16) st[w * 16 + l] = t;
#pragma unroll
        for (int i = 0; i < 8; i++) { a0 = mfma(wg[i], af[i], i ? a0 : f32x16{}); a1 = mfma(wg[8 + i], af[i], i ? a1 : f32x16{}); mx = chk4(mx, af[i]); asm volatile("" : "+v"(mx)); __builtin_amdgcn_sched_barrier(0); }
        if (!__any(mx == SENT) || tries >= RETRY_CAP) break;
      }
      f32x8 m = reduce4(smem, a0, a1, kq, tq, l);
      {
        const f32x2* st2 = (const f32x2*)(smem + L_ST4) + tq * 128 + r;
        const f32x2 sm = (st2[0] + st2[32]) + (st2[64] + st2[96]);
        const float mean = sm[0] * (1.f / D);
        const float var = fmaxf(sm[1] * (1.f / D) - mean * mean, 0.f);
        rstd = __builtin_amdgcn_rsqf(var + LN_EPS);
        mr = -mean * rstd;
      }
      const f32x4 cq0 = *(const f32x4*)(csb + 96 + kq * 16 + 4 * h), cq1 = *(const f32x4*)(csb + 96 + kq * 16 + 8 + 4 * h);
      const float rq = rstd * QSCALE, mq = mr * QSCALE;
#pragma unroll
      for (int e = 0; e < 4; e++) { m[e] = m[e] * rq + mq * cq0[e]; m[4 + e] = m[4 + e] * rq + mq * cq1[e]; }
      ((u32x4*)(smem + L_QX))[w * 64 + l] = pack8(m);
    }
    }
    {
    IDS;
    if (w >= 6) poll_flags(rs, fl + FL_KV + ((b & 15) * 2 + (w - 6)) * 16, 64, l);
    __syncthreads();
    __builtin_amdgcn_s_setprio(0);
    {
      const int kb = OFF_KV + L * 131072 + w * 1024;
      const unsigned lk = lds0 + L_K + w * 1024;
      u32x4 k0w;
      asm volatile("buffer_load_dwordx4 %0, %1, %2, %3 offen sc1" : "=v"(k0w) : "v"(l16), "s"(rs), "s"(OFF_KV + L * 131072 + kq * 1024) : "memory");
#pragma unroll
      for (int c = 0; c < 8; c += 4) {
        dma16_sc1(rs, l16, kb + ((c + idx) & 7) * 8192, lk + c * 8192);
        dma16_sc1(rs, l16, kb + ((c + idx) & 7) * 8192 + 65536, lk + c * 8192 + 65536);
      }
      const u32x4 qa = ((const u32x4*)(smem + L_QX))[w * 64 + l], qb = ((const u32x4*)(smem + L_QX))[(w ^ 4) * 64 + l];
      asm volatile("s_waitcnt vmcnt(4)" : "+v"(k0w) :: "memory");
      float m;
      {
        const f32x16 s0 = mfma(k0w, qa, f32x16{}), s1 = mfma(k0w, qb, f32x16{});
        m = fmaxf(s0[0], s1[0]);
#pragma unroll
        for (int i = 1; i < 16; i++) m = fmaxf(fmaxf(m, s0[i]), s1[i]);
        m = xhalf_max(m);
      }
      const bool vl = r < 16;
      const unsigned ka0 = lds0 + L_K + l * 16;
      const unsigned va0 = vl ? lds0 + L_V + (h * 16 + r) * 16 : lds0 + L_ONE + (r & 15) * 16;
      const unsigned vs = vl ? 2048u : 0u;
      unsigned ka = ka0 + tq * 32768, va = va0 + (vl ? tq * 32768 : 0);
      AttState st;
      f32x16 negm;
#pragma unroll
      for (int i = 0; i < 16; i++) negm[i] = -m;
      asm volatile("s_waitcnt vmcnt(0)\n\ts_barrier" : "+" ATT_NM(negm) :: "memory");
      if (w >= 4) __builtin_amdgcn_s_setprio(1);
      attn_p(st, ka, va, qa, qb, negm);
      attn_a(st, ka, va, vs, qa, qb, negm, rs, l16, kb, idx, lk);
      if (w >= 4) { __builtin_amdgcn_s_setprio(1); attn_b(st, ka, va, vs, qa, qb, negm, 13 - 1); __builtin_amdgcn_s_setprio(0); }
      ST_SC1(SENT4, OFF_OB + L * IMG + myfrag);
#pragma unroll
      for (int i = 0; i < 16; i++) wf[i] = LD_WF(OFF_WOB, i);
      if (L == 0) {
        if (w < 4) { ST_SC1(SENT4, OFF_OA + IMG + oafrag); ST_SC1(SENT4, OFF_OA + IMG + oafrag + 1024); }
        else if (tid < 256 + 136) ST_SC1(SENT4, OFF_SLAB + SLAB_L + slaboff);
        ST_SC1(SENT4, OFF_X1 + IMG + myfrag);
        ST_SC1(SENT4, OFF_X + IMG + myfrag);
        if (l < 16) { ST_SC1(SENT4, OFF_RST + RST_L + (myfrag >> 2)); ST_SC1(SENT4, OFF_RST + 3 * RST_L + (myfrag >> 2)); }
#pragma unroll
        for (int i = 0; i < 12; i++) w1[i] = LD_PL(w1off + (i % 3) * 16 * 32768 + (i / 3) * 1024);
      } else {
#pragma unroll
        for (int i = 0; i < 12; i++) w1[i] = u32x4{};
      }
      attn_b(st, ka, va, vs, qa, qb, negm, w >= 4 ? 1 : 13);
      attn_e(st);
      {
      IDS;
      f32x8 o; float ls;
      {
        f32x4* ox = (f32x4*)(smem + L_OX) + l;
        float* lx = (float*)(smem + L_QX) + l;
        ox[(w * 2) * 64] = f32x4{st.ob[0], st.ob[1], st.ob[2], st.ob[3]}; ox[(w * 2 + 1) * 64] = f32x4{st.ob[4], st.ob[5], st.ob[6], st.ob[7]};
        lx[w * 64] = st.ob[8]; ls = st.oa[8];
#pragma unroll
        for (int e = 0; e < 8; e++) o[e] = st.oa[e];
        __syncthreads();
        const f32x4 u0 = ox[((w ^ 4) * 2) * 64], u1 = ox[((w ^ 4) * 2 + 1) * 64];
        ls += lx[(w ^ 4) * 64];
#pragma unroll
        for (int e = 0; e < 4; e++) { o[e] += u0[e]; o[4 + e] += u1[e]; }
      }
      float chk = fabsf(o[0]);
#pragma unroll
      for (int e = 1; e < 8; e++) chk = fmaxf(chk, fabsf(o[e]));
      chk = fmaxf(chk * 0x1p-27f, ls);
      if (__builtin_expect(__any(!(chk < 0x1p100f) || !(ls >= 0x1p-80f)), 0)) {
        const lds_frag_ptr kp = (lds_frag_ptr)(smem + L_K) + l;
        const u32x4 qf = qa;
        float mx = -INFINITY;
#pragma unroll 1
        for (int t = 0; t < 64; t++) {
          const f32x16 s = mfma(kp[t * 64], qf, f32x16{});
#pragma unroll
          for (int i = 0; i < 16; i++) mx = fmaxf(mx, s[i]);
        }
        mx = xhalf_max(mx);
#pragma unroll
        for (int i = 0; i < 16; i++) negm[i] = -mx;
        const f32x16 of = attn_full(lds0 + L_K + l * 16, r < 16 ? lds0 + L_V + (h * 16 + r) * 16 : lds0 + L_ONE + (r & 15) * 16, r < 16 ? 2048u : 0u, qf, negm);
        ls = of[8];
#pragma unroll
        for (int e = 0; e < 8; e++) o[e] = of[e];
      }
      const float rc = __builtin_amdgcn_rcpf(ls);
#pragma unroll
      for (int e = 0; e < 8; e++) o[e] *= rc;
      DRAIN();
      ST_SC1(pack8(o), OFF_OB + L * IMG + myfrag);
      }
    }
    __syncthreads();
    if (tid == 0) set_flag(rs, fl + FL_OB + rb * 2, j);
    }
    {
    IDS;
    if (L == 0) {
#pragma unroll
      for (int i = 0; i < 12; i++) ((u32x4*)(smem + L_W1))[((i % 3) * 32 + 4 * w + i / 3) * 64 + l] = w1[i];
    }
    if (w == 7) poll_flags(rs, fl + FL_OB + rb * 2, 8, l);
    __syncthreads();
    __builtin_amdgcn_s_setprio(0);
    {
      f32x16 a0, a1;
#pragma unroll 1
      for (unsigned tries = 0;; ++tries) {
        u32x4 af[8];
        unsigned mx = 0u;
#pragma unroll
        for (int i = 0; i < 8; i++) af[i] = LD_SC1(OFF_OB + L * IMG + afrag0 + i * 1024);
        __builtin_amdgcn_sched_barrier(0);
#pragma unroll
        for (int i = 0; i < 8; i++) { a0 = mfma(wf[i], af[i], i ? a0 : f32x16{}); a1 = mfma(wf[8 + i], af[i], i ? a1 : f32x16{}); mx = chk4(mx, af[i]); asm volatile("" : "+v"(mx)); __builtin_amdgcn_sched_barrier(0); }
        if (!__any(mx == SENT) || tries >= RETRY_CAP) break;
      }
      f32x8 m = reduce4(smem, a0, a1, kq, tq, l);
#pragma unroll
      for (int e = 0; e < 4; e++) { m[2 * e] += bflo(xres[e]); m[2 * e + 1] += bfhi(xres[e]); }
      xres = pack8(m);
      if (L == 0) {
        ST_SC1(xres, OFF_X + IMG + myfrag);
        const u32x2 fs = frag_stats(xres);
        if (h == 0) __builtin_amdgcn_raw_buffer_store_b64(fs, rs, l * 8, OFF_RST + RST_L + (myfrag >> 2), 16);
        __syncthreads();
        if (tid == 0) set_flag(rs, FL_X2 + (rb >> 1) * 4, (rb & 1) * 8 + j);
      } else {
        auto s0 = __builtin_amdgcn_permlane32_swap(xres[0], xres[2], false, false);
        auto s1 = __builtin_amdgcn_permlane32_swap(xres[1], xres[3], false, false);
        const auto ro = __builtin_amdgcn_make_buffer_rsrc(outp, 0, S * D * 2, 0x00020000);
        __builtin_amdgcn_raw_buffer_store_b128(u32x4{s0[0], s1[0], s0[1], s1[1]}, ro, r * 1024 + h * 16, (rb * 2 + tq) * 32768 + (4 * j + kq) * 32, 0);
      }
    }
    }
  }
}

extern "C" void kernel_launch(void* const* d_in, const int* in_sizes, int n_in, void* d_out, int out_size,
                              void* d_ws, size_t ws_size, hipStream_t stream) {
  char* ws = (char*)d_ws;
  (void)hipFuncSetAttribute((const void*)k_prep, hipFuncAttributeMaxDynamicSharedMemorySize, P_BYTES);
  (void)hipFuncSetAttribute((const void*)k_main, hipFuncAttributeMaxDynamicSharedMemorySize, L_BYTES);
  k_prep<<<193 + 128 + 16 + 2 * FL_LAYER / 64, 512, P_BYTES, stream>>>((const uint16_t*)d_in[0], (const uint16_t*)d_in[1], (const uint16_t*)d_in[2],
                                            (const uint16_t*)d_in[4], (const uint16_t*)d_in[5], (const uint16_t*)d_in[6],
                                            (const uint16_t*)d_in[7], ws);
  k_main<<<256, 512, L_BYTES, stream>>>(ws, (u32x4*)d_out);
}
```
